```python
import torch

def _softmax(x, axis=-1):
    return torch.softmax(x, dim=axis)
def _gelu(x):
    return torch.nn.functional.gelu(x, approximate="tanh")
def _silu(x):
    return torch.nn.functional.silu(x)
def _sigmoid(x):
    return torch.sigmoid(x)
def _layernorm(x, axis=-1, eps=1e-5):
    m = x.mean(dim=axis, keepdim=True)
    v = x.var(dim=axis, keepdim=True, unbiased=False)
    return (x - m) / torch.sqrt(v + eps)
def _scatter_add(tgt, idx, src, axis=0):
    return tgt.clone().index_add_(axis, idx, src)
def _segment_sum(src, seg, n):
    out = torch.zeros((n,) + src.shape[1:], dtype=src.dtype)
    return out.index_add_(0, seg, src)
def _pad(x, pw):
    flat = tuple(v for pair in reversed(pw) for v in pair)
    return torch.nn.functional.pad(x, flat)

kernel_name = 'sequential_preLN__ssm_conv_h3__glu_gated_swiglu__prompt'

def setup_inputs():

    _w = (lambda a: (lambda t: t.to(torch.bfloat16) if t.is_floating_point() else t)(torch.as_tensor(a)))
    def _A_setup():
        B, S, D = 1, 2048, 448
        ks = 4
        return {
            "q": _w((lambda *s: torch.randn(*s, dtype=torch.bfloat16) * 0.05)(B, S, D)),
            "k": _w((lambda *s: torch.randn(*s, dtype=torch.bfloat16) * 0.2)(B, S, D)),
            "v": _w((lambda *s: torch.randn(*s, dtype=torch.bfloat16) * 0.2)(B, S, D)),
            "shift_k": _w((lambda *s: torch.randn(*s, dtype=torch.bfloat16) * 0.2)(D, ks) * 0.5),
            "ssm_k": _w((lambda *s: torch.randn(*s, dtype=torch.bfloat16) * 0.4)(D, S) * (1.0 / S ** 0.5)),
            "ssm_D": _w((lambda *s: torch.randn(*s, dtype=torch.bfloat16) * 0.2)(D)),
        }
    _A_in = _A_setup()

    def _B_setup():
        B, S, D, F = 1, 2048, 448, 1792
        return {
            "x": _w((lambda *s: torch.randn(*s, dtype=torch.bfloat16) * 0.05)(B, S, D)),
            "w1": _w((lambda *s: torch.randn(*s, dtype=torch.bfloat16) * 0.2)(D, F) * (2.0 / D) ** 0.5),
            "w3": _w((lambda *s: torch.randn(*s, dtype=torch.bfloat16) * 0.2)(D, F) * (2.0 / D) ** 0.5),
            "w2": _w((lambda *s: torch.randn(*s, dtype=torch.bfloat16) * 0.2)(F, D) * (2.0 / F) ** 0.5),
        }
    _B_in = _B_setup()

    return {
        "A_q": _A_in["q"],
        "A_k": _A_in["k"],
        "A_v": _A_in["v"],
        "A_shift_k": _A_in["shift_k"],
        "A_ssm_k": _A_in["ssm_k"],
        "A_ssm_D": _A_in["ssm_D"],
        "B_x": _B_in["x"],
        "B_w1": _B_in["w1"],
        "B_w3": _B_in["w3"],
        "B_w2": _B_in["w2"],
    }

def reference(A_q, A_k, A_v, A_shift_k, A_ssm_k, A_ssm_D, B_x, B_w1, B_w3, B_w2):
    def _A_ref(q, k, v, shift_k, ssm_k, ssm_D):
        B, S, D = q.shape
        ks = shift_k.shape[-1]
        k_pad = _pad(k, ((0, 0), (ks - 1, 0), (0, 0)))
        k_shift = torch.zeros_like(k)
        for t in range(ks):
            k_shift = k_shift + k_pad[:, t:t + S, :] * shift_k[:, ks - 1 - t][None, None, :]
        kv = k_shift * v
        L = 2 * S
        kv_pad = _pad(kv, ((0, 0), (0, S), (0, 0)))
        kf_pad = _pad(ssm_k, ((0, 0), (0, S)))
        kv_f = (lambda x, axes=(-2,-1): torch.fft.rfft2(x.float(), dim=axes))(kv_pad[:, None, :, :], axes=(1, 2))
        kf_f = (lambda x, axes=(-2,-1): torch.fft.rfft2(x.float(), dim=axes))(kf_pad[:, None, :], axes=(1, 2))
        kf_f = (lambda x, *d: x.permute(*d))(kf_f, 1, 2, 0)
        y_f = kv_f * kf_f[None, :, :, :]
        y = (lambda x, s, axes=(-2,-1): torch.fft.irfft2(x, s=s, dim=axes).to(torch.bfloat16))(y_f, (1, L), axes=(1, 2))[:, 0, :S, :]
        y = y + kv * ssm_D[None, None, :]
        return q * y

    def _B_ref(x, w1, w3, w2):
        gate = torch.einsum("bsd,df->bsf", x, w1)
        up = torch.einsum("bsd,df->bsf", x, w3)
        h = _silu(gate) * up
        out = torch.einsum("bsf,fd->bsd", h, w2)
        return out

    x = A_q
    h = x + _A_ref(_layernorm(x, axis=-1), A_k, A_v, A_shift_k, A_ssm_k, A_ssm_D)
    h2 = h + _B_ref(_layernorm(h, axis=-1), B_w1, B_w3, B_w2)
    return h2
```

```cpp
#include <hip/hip_runtime.h>
#include <hip/hip_bf16.h>
typedef unsigned short u16;
constexpr int S=2048, D=448, F=1792;
__device__ __forceinline__ float b2f(u16 b){ return __builtin_bit_cast(float,(unsigned)b<<16);}
__device__ __forceinline__ u16 f2b(float f){ return __builtin_bit_cast(u16, __float2bfloat16(f)); }
__device__ __forceinline__ float rb(float f){ return b2f(f2b(f)); }

__global__ void kA(const u16* x,const u16* k,const u16* v,const u16* sk,u16* qln,u16* kv){
  int s=blockIdx.x, t=threadIdx.x;
  float xs[7]; float sum=0;
  for(int i=0;i<7;i++){ xs[i]=b2f(x[s*D+t+64*i]); sum+=xs[i]; }
  for(int o=32;o;o>>=1) sum+=__shfl_xor(sum,o);
  float m=sum/D, var=0;
  for(int i=0;i<7;i++){ float d=xs[i]-m; var+=d*d; }
  for(int o=32;o;o>>=1) var+=__shfl_xor(var,o);
  float r=rsqrtf(var/D+1e-5f);
  for(int i=0;i<7;i++){ int d=t+64*i; qln[s*D+d]=f2b((xs[i]-m)*r);
    float a=0; for(int j=0;j<4;j++){ if(s-j>=0) a+=b2f(k[(s-j)*D+d])*b2f(sk[d*4+j]); }
    kv[s*D+d]=f2b(rb(a)*b2f(v[s*D+d])); }
}
__global__ void kB(const u16* kv,const u16* ssk,const u16* sD,u16* yt){
  int d=blockIdx.y*64+threadIdx.x, s=blockIdx.x;
  float a=0; for(int j=0;j<=s;j++) a+=b2f(kv[(s-j)*D+d])*b2f(ssk[d*S+j]);
  yt[s*D+d]=f2b(rb(a)+rb(b2f(kv[s*D+d])*b2f(sD[d])));
}
__global__ void kC(const u16* x,const u16* qln,const u16* yt,u16* h,u16* hln){
  int s=blockIdx.x, t=threadIdx.x;
  float xs[7]; float sum=0;
  for(int i=0;i<7;i++){ int d=s*D+t+64*i; xs[i]=rb(b2f(x[d])+rb(b2f(qln[d])*b2f(yt[d]))); h[d]=f2b(xs[i]); sum+=xs[i]; }
  for(int o=32;o;o>>=1) sum+=__shfl_xor(sum,o);
  float m=sum/D, var=0;
  for(int i=0;i<7;i++){ float d=xs[i]-m; var+=d*d; }
  for(int o=32;o;o>>=1) var+=__shfl_xor(var,o);
  float r=rsqrtf(var/D+1e-5f);
  for(int i=0;i<7;i++){ hln[s*D+t+64*i]=f2b((xs[i]-m)*r); }
}
__global__ void kD(const u16* hln,const u16* w1,const u16* w3,u16* H){
  int f=blockIdx.y*64+threadIdx.x, s=blockIdx.x;
  float g=0,u=0; for(int d=0;d<D;d++){ float a=b2f(hln[s*D+d]); g+=a*b2f(w1[d*F+f]); u+=a*b2f(w3[d*F+f]); }
  g=rb(g); u=rb(u);
  H[s*F+f]=f2b(rb(g/(1.f+__expf(-g)))*u);
}
__global__ void kE(const u16* H,const u16* w2,const u16* h,u16* out){
  int d=blockIdx.y*64+threadIdx.x, s=blockIdx.x;
  float a=0; for(int f=0;f<F;f++) a+=b2f(H[s*F+f])*b2f(w2[f*D+d]);
  out[s*D+d]=f2b(b2f(h[s*D+d])+rb(a));
}
extern "C" void kernel_launch(void* const* d_in, const int* in_sizes, int n_in,
                              void* d_out, int out_size, void* d_ws, size_t ws_size,
                              hipStream_t stream) {
  const u16* x=(const u16*)d_in[0],*k=(const u16*)d_in[1],*v=(const u16*)d_in[2],*sk=(const u16*)d_in[3],*ssk=(const u16*)d_in[4],*sD=(const u16*)d_in[5];
  const u16* w1=(const u16*)d_in[7],*w3=(const u16*)d_in[8],*w2=(const u16*)d_in[9];
  u16* ws=(u16*)d_ws; u16* qln=ws,*kv=ws+S*D,*yt=ws+2*S*D,*h=ws+3*S*D,*hln=ws+4*S*D,*H=ws+5*S*D;
  kA<<<S,64,0,stream>>>(x,k,v,sk,qln,kv);
  kB<<<dim3(S,7),64,0,stream>>>(kv,ssk,sD,yt);
  kC<<<S,64,0,stream>>>(x,qln,yt,h,hln);
  kD<<<dim3(S,F/64),64,0,stream>>>(hln,w1,w3,H);
  kE<<<dim3(S,7),64,0,stream>>>(H,w2,h,(u16*)d_out);
}
```

```cpp
#include <hip/hip_runtime.h>
#include <hip/hip_bf16.h>

typedef unsigned u32;
typedef unsigned short u16;
typedef unsigned long long u64;
typedef u32 v4u __attribute__((ext_vector_type(4)));
typedef u32 v2u __attribute__((ext_vector_type(2)));
typedef float v4f __attribute__((ext_vector_type(4)));
typedef float v2f __attribute__((ext_vector_type(2)));
typedef float v16f __attribute__((ext_vector_type(16)));
typedef __bf16 bf8 __attribute__((ext_vector_type(8)));
typedef short s4 __attribute__((ext_vector_type(4)));
typedef __amdgpu_buffer_rsrc_t rsrc_t;
#define LDSP(T, off) ((__attribute__((address_space(3))) T*)(size_t)(off))

constexpr int S = 2048, D = 448, F = 1792;
constexpr u32 WS_EPOCH = 0;
constexpr u32 WS_WFLAG = 4096;
constexpr u32 WS_KVG = 8192;
constexpr u32 WS_XQG = WS_KVG + 256u * 224 * 64;
constexpr u32 WS_T13 = WS_XQG + 256u * 224 * 128;
constexpr u32 WS_HT = WS_T13 + 14u * 8 * 28 * 1024;
constexpr u32 WS_HF = WS_HT + 32u * 448 * 128;
constexpr u32 WS_END = WS_HF + 64u * 112 * 1024;
constexpr u32 WS_DBG = WS_END;
constexpr u32 L_TAP = 0;
constexpr u32 L_RX = 66560;
constexpr u32 L_RED = L_RX + 9216;
constexpr u32 L_STAT = L_RED + 65536;
constexpr u32 L_P0KV = L_RED, L_P0X = L_RED + 8192;
constexpr u32 L_H = 0;
constexpr u32 L_PART = 131072;
constexpr u32 L_CS = L_PART + 8192;
constexpr u32 L_TOTAL = 144 * 1024;
constexpr u32 OOB = 0x80000000u;
constexpr int SC1 = 16;
constexpr u32 SPIN_MAX = 1u << 16;

#define FENCE() asm volatile("" ::: "memory")
#define WAITV(n) asm volatile("s_waitcnt vmcnt(" #n ")" ::: "memory")
#define BAR() do { asm volatile("s_waitcnt lgkmcnt(0)" ::: "memory"); __builtin_amdgcn_s_barrier(); FENCE(); } while (0)

__device__ __forceinline__ rsrc_t mk(const void* p, u32 bytes) { return __builtin_amdgcn_make_buffer_rsrc((void*)p, 0, bytes, 0x00020000); }
__device__ __forceinline__ float lo2f(u32 v) { return __builtin_bit_cast(float, v << 16); }
__device__ __forceinline__ float hi2f(u32 v) { return __builtin_bit_cast(float, v & 0xffff0000u); }
typedef __bf16 bf2 __attribute__((ext_vector_type(2)));
__device__ __forceinline__ u32 pk(float a, float b) { return __builtin_bit_cast(u32, __builtin_convertvector(v2f{a, b}, bf2)); }
__device__ __forceinline__ u32 plo(u32 a, u32 b) { return __builtin_amdgcn_perm(b, a, 0x05040100u); }
__device__ __forceinline__ u32 phi(u32 a, u32 b) { return __builtin_amdgcn_perm(b, a, 0x07060302u); }
__device__ __forceinline__ u32 canon(u32 v) { return min(v, 0xFFFFFFFEu); }
__device__ __forceinline__ u32 max4(v4u t) { return max(max(t.x, t.y), max(t.z, t.w)); }
__device__ __forceinline__ bf8 asbf(v4u v) { return __builtin_bit_cast(bf8, v); }
__device__ __forceinline__ v2u trd(u32 a) { return __builtin_bit_cast(v2u, __builtin_amdgcn_ds_read_tr16_b64_v4i16(LDSP(s4, a))); }
__device__ __forceinline__ void st128(v4u v, rsrc_t r, u32 off) { __builtin_amdgcn_raw_buffer_store_b128(v, r, off, 0, SC1); }
__device__ __forceinline__ v4u ld128(rsrc_t r, u32 off, u32 so) { return __builtin_amdgcn_raw_buffer_load_b128(r, off, so, SC1); }
__device__ __forceinline__ u32 f2u(float v) { return __builtin_bit_cast(u32, v); }
__device__ __forceinline__ float u2f(u32 v) { return __builtin_bit_cast(float, v); }
template <int CTRL> __device__ __forceinline__ float dppadd(float v) { return v + u2f(__builtin_amdgcn_update_dpp(0, f2u(v), CTRL, 0xF, 0xF, true)); }
__device__ __forceinline__ float swap16add(float a, float b) { const auto r = __builtin_amdgcn_permlane16_swap(f2u(a), f2u(b), false, false); return u2f(r[0]) + u2f(r[1]); }
__device__ __forceinline__ float swap32add(float a, float b) { const auto r = __builtin_amdgcn_permlane32_swap(f2u(a), f2u(b), false, false); return u2f(r[0]) + u2f(r[1]); }
__device__ __forceinline__ float wsum(float v) {
  v = dppadd<0xB1>(v); v = dppadd<0x4E>(v); v = dppadd<0x141>(v); v = dppadd<0x140>(v);
  v = swap16add(v, v);
  return swap32add(v, v);
}
#define SCHED() __builtin_amdgcn_sched_barrier(0)
#define PHASE() do { SCHED(); asm volatile("" : "+v"(tid)); lane = tid & 63; SCHED(); } while (0)
#define STAMP(i)

__global__ void __launch_bounds__(512, 2)
fused_kernel(const u16* __restrict__ gx, const u16* __restrict__ gk, const u16* __restrict__ gv, const u16* __restrict__ gsk,
             const u16* __restrict__ gssm, const u16* __restrict__ gsd, const u16* __restrict__ gw1, const u16* __restrict__ gw3,
             const u16* __restrict__ gw2, u16* __restrict__ gout, unsigned char* __restrict__ ws) {
  int tid = threadIdx.x, lane = tid & 63;
  const int w = __builtin_amdgcn_readfirstlane(tid >> 6), b = blockIdx.x;
  const int xc = b & 7, ii = b >> 3;
  const bool mainwg = ii < 28;
  const int p = xc * 28 + ii;
  const int rt = 2 * xc + ii / 14, fj = ii % 14;
  const rsrc_t rws = mk(ws, WS_END);
  STAMP(0);

  v2u ep = __builtin_amdgcn_raw_buffer_load_b64(rws, WS_EPOCH, 0, SC1);
  const rsrc_t rx = mk(gx, S * D * 2);
  v4u srow = __builtin_amdgcn_raw_buffer_load_b128(rx, lane < 56 ? (u32)((8 * b + w) * 896 + lane * 16) : OOB, 0, 0);
  const int pp = tid & 255;
  const u32 colo = pp < 224 ? (u32)(8 * b * 896 + pp * 4) : OOB;
  u32 ra[11], rb[8];
  v4u skw;
  if (w < 4) {
    const rsrc_t rk = mk(gk, S * D * 2), rv = mk(gv, S * D * 2), rs = mk(gsk, D * 8);
    const u32 halo = (b > 0 && pp < 224) ? colo - 3 * 896 : OOB;
#pragma unroll
    for (int i = 0; i < 3; i++) ra[i] = __builtin_amdgcn_raw_buffer_load_b32(rk, halo + i * 896, 0, 0);
#pragma unroll
    for (int i = 0; i < 8; i++) ra[3 + i] = __builtin_amdgcn_raw_buffer_load_b32(rk, colo, i * 896, 0);
#pragma unroll
    for (int i = 0; i < 8; i++) rb[i] = __builtin_amdgcn_raw_buffer_load_b32(rv, colo, i * 896, 0);
    skw = __builtin_amdgcn_raw_buffer_load_b128(rs, pp * 16, 0, 0);
  } else {
#pragma unroll
    for (int i = 0; i < 8; i++) rb[i] = __builtin_amdgcn_raw_buffer_load_b32(rx, colo, i * 896, 0);
  }
  FENCE();
  const v4u poison = {0xFFFFFFFFu, 0xFFFFFFFFu, 0xFFFFFFFFu, 0xFFFFFFFFu};
  st128(poison, rws, mainwg ? WS_HT + (tid >> 4) * 57344 + p * 256 + (tid & 15) * 16 : OOB);
  FENCE();
  v4u ta, tb;
  {
    const rsrc_t rt_ = mk(gssm, D * S * 2);
    const u32 o = (u32)((2 * p + (tid >> 8)) * 4096 + pp * 16);
    ta = __builtin_amdgcn_raw_buffer_load_b128(rt_, (mainwg && pp > 0) ? o - 16 : OOB, 0, 0);
    tb = __builtin_amdgcn_raw_buffer_load_b128(rt_, mainwg ? o : OOB, 0, 0);
  }
  const int u = 255 - b, ukb = u / 14, uj = u % 14;
  u32 wr[8];
  {
    const rsrc_t rw = mk((w & 1) ? gw3 : gw1, D * F * 2);
    const u32 o = u < 196 ? (u32)((32 * ukb + 8 * (w >> 1)) * 3584 + uj * 256 + lane * 4) : OOB;
#pragma unroll
    for (int i = 0; i < 8; i++) wr[i] = __builtin_amdgcn_raw_buffer_load_b32(rw, o, i * 3584, 0);
  }
  FENCE();
  {
    float e[8] = {lo2f(srow.x), hi2f(srow.x), lo2f(srow.y), hi2f(srow.y), lo2f(srow.z), hi2f(srow.z), lo2f(srow.w), hi2f(srow.w)};
    float s = ((e[0] + e[1]) + (e[2] + e[3])) + ((e[4] + e[5]) + (e[6] + e[7]));
    const float mean = wsum(s) * (1.f / D);
    float q = 0.f;
#pragma unroll
    for (int i = 0; i < 8; i++) { const float d = e[i] - mean; q += d * d; }
    q = wsum(lane < 56 ? q : 0.f);
    const float rstd = rsqrtf(q * (1.f / D) + 1e-5f);
    if (lane == 0) *LDSP(v2f, L_STAT + w * 8) = v2f{mean, rstd};
  }
  const u32 t0 = __builtin_amdgcn_readfirstlane(ep.x) + 1u, t1 = __builtin_amdgcn_readfirstlane(ep.y) ^ 0x5bd1e995u;
  if (w < 4) {
    const float s0[4] = {lo2f(skw.x), hi2f(skw.x), lo2f(skw.y), hi2f(skw.y)};
    const float s1[4] = {lo2f(skw.z), hi2f(skw.z), lo2f(skw.w), hi2f(skw.w)};
    float k0[8], k1[8];
#pragma unroll
    for (int i = 0; i < 8; i++) {
      float a0 = lo2f(ra[i + 3]) * s0[0], a1 = hi2f(ra[i + 3]) * s1[0];
#pragma unroll
      for (int j = 1; j < 4; j++) { a0 += lo2f(ra[i + 3 - j]) * s0[j]; a1 += hi2f(ra[i + 3 - j]) * s1[j]; }
      k0[i] = a0 * lo2f(rb[i]); k1[i] = a1 * hi2f(rb[i]);
    }
    *LDSP(v4u, L_P0KV + pp * 32) = v4u{pk(k0[3], k0[2]), pk(k0[1], k0[0]), pk(k0[7], k0[6]), pk(k0[5], k0[4])};
    *LDSP(v4u, L_P0KV + pp * 32 + 16) = v4u{pk(k1[3], k1[2]), pk(k1[1], k1[0]), pk(k1[7], k1[6]), pk(k1[5], k1[4])};
  } else {
    *LDSP(v4u, L_P0X + pp * 32) = v4u{plo(rb[0], rb[1]), plo(rb[2], rb[3]), plo(rb[4], rb[5]), plo(rb[6], rb[7])};
    *LDSP(v4u, L_P0X + pp * 32 + 16) = v4u{phi(rb[0], rb[1]), phi(rb[2], rb[3]), phi(rb[4], rb[5]), phi(rb[6], rb[7])};
  }
  WAITV(10);
  BAR();
  STAMP(1);
  {
    const v2u p0 = *LDSP(v2u, L_P0KV + tid * 8), p1 = *LDSP(v2u, L_P0KV + 4096 + tid * 8);
    const u32 o = WS_KVG + b * 14336 + tid * 16;
    st128(v4u{p0.x, p0.y, t0, t1}, rws, o);
    st128(v4u{p1.x, p1.y, t0, t1}, rws, tid < 384 ? o + 8192 : OOB);
    const v4f sa = *LDSP(v4f, L_STAT + (tid & 2) * 16), sb = *LDSP(v4f, L_STAT + (tid & 2) * 16 + 16);
    const u32 ox = WS_XQG + b * 28672 + tid * 16;
#pragma unroll
    for (int i = 0; i < 4; i++) {
      const v2u px = *LDSP(v2u, L_P0X + (tid >> 1) * 8 + i * 2048);
      const u32 qx = pk((lo2f(px.x) - sa.x) * sa.y, (hi2f(px.x) - sa.z) * sa.w), qy = pk((lo2f(px.y) - sb.x) * sb.y, (hi2f(px.y) - sb.z) * sb.w);
      st128(v4u{(tid & 1) ? qx : px.x, (tid & 1) ? qy : px.y, t0, t1}, rws, (i < 3 || tid < 256) ? ox + i * 8192 : OOB);
    }
  }
  FENCE();
  {
    const u32 o = u < 196 ? WS_T13 + (u32)(((uj * 8 + (lane >> 3)) * 28 + 2 * ukb + (w >> 2)) * 1024 + ((lane & 7) + 16 * (w & 1) + 32 * ((w >> 1) & 1)) * 16) : OOB;
    st128(v4u{plo(wr[0], wr[1]), plo(wr[2], wr[3]), plo(wr[4], wr[5]), plo(wr[6], wr[7])}, rws, o);
    st128(v4u{phi(wr[0], wr[1]), phi(wr[2], wr[3]), phi(wr[4], wr[5]), phi(wr[6], wr[7])}, rws, o + 128);
  }
  FENCE();
  if (mainwg) {
    const u32 a[8] = {ta.x, ta.y, ta.z, ta.w, tb.x, tb.y, tb.z, tb.w};
    u32 od[7];
#pragma unroll
    for (int j = 0; j < 7; j++) od[j] = __builtin_amdgcn_alignbit(a[j + 1], a[j], 16);
    const u32 base = L_TAP + (tid >> 8) * 33280 + (pp + 3) * 16;
#pragma unroll
    for (int uu = 0; uu < 4; uu++) {
      *LDSP(v4u, base + (2 * uu) * 4160) = v4u{od[uu], od[uu + 1], od[uu + 2], od[uu + 3]};
      *LDSP(v4u, base + (2 * uu + 1) * 4160) = v4u{a[uu + 1], a[uu + 2], a[uu + 3], a[uu + 4]};
    }
    const v4u z = {0u, 0u, 0u, 0u};
    if (tid < 16) *LDSP(v4u, L_TAP + tid * 4160 + 32) = z;
    if (tid < 64) *LDSP(v4u, L_RX + (tid >> 5) * 4608 + 4096 + (tid & 31) * 16) = z;
  }
  WAITV(0);
  BAR();
  if (tid == 0 && u < 196) __builtin_amdgcn_raw_buffer_store_b64(v2u{t0, t1}, rws, WS_WFLAG + uj * 128 + ukb * 8, 0, SC1);
  STAMP(2);
  PHASE();
  if (!mainwg) return;

  {
    v4u g0, g1;
    const u32 o = WS_KVG + (tid >> 2) * 14336 + p * 64 + (tid & 3) * 16;
    for (u32 spin = 0; spin < SPIN_MAX; spin++) {
      FENCE();
      g0 = __builtin_amdgcn_raw_buffer_load_b128(rws, o, 0, SC1);
      g1 = __builtin_amdgcn_raw_buffer_load_b128(rws, o, 128 * 14336, SC1);
      const bool bad = (g0.z != t0) | (g0.w != t1) | (g1.z != t0) | (g1.w != t1);
      if (!__any(bad)) break;
      __builtin_amdgcn_s_sleep(4);
    }
    const int t = 8 * (tid >> 2) + 4 * (tid & 1);
    const u32 la = L_RX + ((tid >> 1) & 1) * 4608 + (2044 - t) * 2;
    *LDSP(v2u, la) = v2u{g0.x, g0.y};
    *LDSP(v2u, la - 2048) = v2u{g1.x, g1.y};
  }
  const int ch = w >> 2, wq = w & 3, la_ = lane & 15, kg = lane >> 4;
  FENCE();
  v4u xq[4];
  const u32 xqo = WS_XQG + (64 * wq + 2 * la_ + (kg >> 1)) * 28672 + p * 128 + ch * 64 + (kg & 1) * 32;
  xq[0] = __builtin_amdgcn_raw_buffer_load_b128(rws, xqo, 0, SC1);
  xq[1] = __builtin_amdgcn_raw_buffer_load_b128(rws, xqo + 16, 0, SC1);
  xq[2] = __builtin_amdgcn_raw_buffer_load_b128(rws, xqo, 32 * 28672, SC1);
  xq[3] = __builtin_amdgcn_raw_buffer_load_b128(rws, xqo + 16, 32 * 28672, SC1);
  v2u wf = __builtin_amdgcn_raw_buffer_load_b64(rws, WS_WFLAG + fj * 128 + min(lane, 13) * 8, 0, SC1);
  FENCE();
#pragma unroll
  for (int n = 0; n < 4; n++) st128(poison, rws, WS_HF + (u32)(((4 * rt + n) * 112 + 8 * fj + w) * 1024 + lane * 16));
  FENCE();
  const float dval = lo2f(gsd[2 * p + ch]);
  BAR();
  STAMP(3);
  v4f acc[8];
#pragma unroll
  for (int n = 0; n < 8; n++) acc[n] = v4f{0.f, 0.f, 0.f, 0.f};
  v4u w13[28];
#pragma unroll
  for (int e = 0; e < 2; e++) {
    const int r = 4 * wq + 2 * e;
    const u32 aa = L_TAP + ch * 33280 + ((la_ & 7) * 260 + (la_ >> 3) + kg + 2 * (r + 1)) * 16;
    const u32 ba = L_RX + ch * 4608 + (30 + 2 * r - 2 * la_ + kg) * 16;
    v4u A[8], B[8];
#pragma unroll
    for (int m = 0; m < 8; m++) { A[m] = *LDSP(v4u, aa + m * 512); B[m] = *LDSP(v4u, ba + (7 - m) * 512); }
#pragma unroll
    for (int m = 0; m < 8; m++)
#pragma unroll
      for (int n = m; n < 8; n++) acc[n] = __builtin_amdgcn_mfma_f32_16x16x32_bf16(asbf(A[m]), asbf(B[n - m]), acc[n], 0, 0, 0);
    if (e == 0) {
      for (u32 spin = 0; spin < SPIN_MAX; spin++) {
        if (!__any((wf.x != t0) | (wf.y != t1))) break;
        FENCE();
        wf = __builtin_amdgcn_raw_buffer_load_b64(rws, WS_WFLAG + fj * 128 + min(lane, 13) * 8, 0, SC1);
      }
      FENCE();
      const u32 o = WS_T13 + (u32)((fj * 8 + w) * 28 * 1024 + lane * 16);
#pragma unroll
      for (int s = 0; s < 28; s++) w13[s] = __builtin_amdgcn_raw_buffer_load_b128(rws, o, s * 1024, SC1);
      FENCE();
    }
  }
#pragma unroll
  for (int n = 0; n < 8; n++)
    if ((n >> 1) != wq) *LDSP(v4f, L_RED + (w * 8 + n) * 1024 + lane * 16) = acc[n];
  WAITV(28);
  BAR();
  STAMP(4);
  {
    v4f y0, y1;
#pragma unroll
    for (int n = 0; n < 4; n++) if (n == wq) { y0 = acc[2 * n]; y1 = acc[2 * n + 1]; }
#pragma unroll
    for (int sq = 1; sq < 4; sq++) {
      const int src = ch * 4 + ((wq + sq) & 3);
      y0 += *LDSP(v4f, L_RED + (src * 8 + 2 * wq) * 1024 + lane * 16);
      y1 += *LDSP(v4f, L_RED + (src * 8 + 2 * wq + 1) * 1024 + lane * 16);
    }
    const int t = 512 * wq + 16 * la_ + 4 * kg;
    const v2u kv0 = *LDSP(v2u, L_RX + ch * 4608 + (2044 - t) * 2), kv1 = *LDSP(v2u, L_RX + ch * 4608 + (2044 - 256 - t) * 2);
    for (u32 spin = 0; spin < SPIN_MAX; spin++) {
      bool bad = false;
#pragma unroll
      for (int i = 0; i < 4; i++) bad |= (xq[i].z != t0) | (xq[i].w != t1);
      if (!__any(bad)) break;
      FENCE();
      xq[0] = __builtin_amdgcn_raw_buffer_load_b128(rws, xqo, 0, SC1);
      xq[1] = __builtin_amdgcn_raw_buffer_load_b128(rws, xqo + 16, 0, SC1);
      xq[2] = __builtin_amdgcn_raw_buffer_load_b128(rws, xqo, 32 * 28672, SC1);
      xq[3] = __builtin_amdgcn_raw_buffer_load_b128(rws, xqo + 16, 32 * 28672, SC1);
    }
    u32 h0x = pk(lo2f(xq[0].x) + lo2f(xq[1].x) * (y0.x + hi2f(kv0.y) * dval), hi2f(xq[0].x) + hi2f(xq[1].x) * (y0.y + lo2f(kv0.y) * dval));
    u32 h0y = pk(lo2f(xq[0].y) + lo2f(xq[1].y) * (y0.z + hi2f(kv0.x) * dval), hi2f(xq[0].y) + hi2f(xq[1].y) * (y0.w + lo2f(kv0.x) * dval));
    u32 h1x = pk(lo2f(xq[2].x) + lo2f(xq[3].x) * (y1.x + hi2f(kv1.y) * dval), hi2f(xq[2].x) + hi2f(xq[3].x) * (y1.y + lo2f(kv1.y) * dval));
    u32 h1y = pk(lo2f(xq[2].y) + lo2f(xq[3].y) * (y1.z + hi2f(kv1.x) * dval), hi2f(xq[2].y) + hi2f(xq[3].y) * (y1.w + lo2f(kv1.x) * dval));
    const auto sx = __builtin_amdgcn_permlane16_swap(canon(h0x), canon(h1x), false, false);
    const auto sy = __builtin_amdgcn_permlane16_swap(canon(h0y), canon(h1y), false, false);
    const int tt = 512 * wq + 256 * (kg & 1) + 16 * la_ + 4 * (kg & 2);
    st128(v4u{sx[0], sy[0], sx[1], sy[1]}, rws, WS_HT + (tt >> 6) * 57344 + (2 * p + ch) * 128 + (tt & 63) * 2);
  }
  FENCE();
  STAMP(5);
  PHASE();

  {
    float cs = 0.f;
#pragma unroll
    for (int s = 0; s < 28; s++) {
      const v4u v = w13[s];
      cs += ((lo2f(v.x) + hi2f(v.x)) + (lo2f(v.y) + hi2f(v.y))) + ((lo2f(v.z) + hi2f(v.z)) + (lo2f(v.w) + hi2f(v.w)));
    }
    cs = swap32add(cs, cs);
    BAR();
    if (lane < 32) *LDSP(float, L_CS + w * 128 + lane * 4) = cs;
  }
  {
    v4u hc[14];
    const u32 o = WS_HT + rt * 114688 + tid * 16;
#pragma unroll
    for (int i = 0; i < 14; i++) hc[i] = __builtin_amdgcn_raw_buffer_load_b128(rws, o, i * 8192, SC1);
    for (u32 spin = 0; spin < SPIN_MAX; spin++) {
      u32 badmask = 0;
#pragma unroll
      for (int i = 0; i < 14; i++) if (__any(max4(hc[i]) == 0xFFFFFFFFu)) badmask |= 1u << i;
      if (!badmask) break;
      FENCE();
#pragma unroll
      for (int i = 0; i < 14; i++) if (badmask >> i & 1) hc[i] = __builtin_amdgcn_raw_buffer_load_b128(rws, o, i * 8192, SC1);
    }
    STAMP(6);
    const u32 lw = L_H + (tid >> 3) * 128 + (((tid & 7) ^ (((tid >> 4) & 1) << 2)) * 16);
#pragma unroll
    for (int i = 0; i < 14; i++) *LDSP(v4u, lw + i * 8192) = hc[i];
#pragma unroll
    for (int hf = 0; hf < 2; hf++) {
      float sm[8], sq[8];
#pragma unroll
      for (int j = 0; j < 8; j++) sm[j] = sq[j] = 0.f;
#pragma unroll
      for (int i = 0; i < 7; i++) {
        const v4u v = hc[hf * 7 + i];
        const float e[8] = {lo2f(v.x), hi2f(v.x), lo2f(v.y), hi2f(v.y), lo2f(v.z), hi2f(v.z), lo2f(v.w), hi2f(v.w)};
#pragma unroll
        for (int j = 0; j < 8; j++) { sm[j] += e[j]; sq[j] += e[j] * e[j]; }
      }
      float uu[8], vv[4];
#pragma unroll
      for (int j = 0; j < 8; j++) uu[j] = swap32add(sm[j], sq[j]);
#pragma unroll
      for (int j = 0; j < 4; j++) vv[j] = dppadd<0x128>(swap16add(uu[j], uu[j + 4]));
      if (!(lane & 8)) *LDSP(v4f, L_PART + ((hf * 8 + w) * 128 + (lane >> 5) * 64 + (lane & 7) * 8 + ((lane >> 4) & 1) * 4) * 4) = v4f{vv[0], vv[1], vv[2], vv[3]};
      SCHED();
    }
  }
  BAR();
  STAMP(7);
  PHASE();
  int hh = lane >> 5;
  const u32 hfo = WS_HF + (u32)((4 * rt * 112 + 8 * fj + w) * 1024 + lane * 16);
  const u32 trb = L_H + (8 * hh + ((lane & 15) >> 2)) * 128 + ((lane >> 4) & 1) * 32 + (lane & 3) * 8;
  const u32 swz = ((lane >> 3) & 1) * 64;
  float csv[16];
#pragma unroll
  for (int g = 0; g < 4; g++) {
    const v4f c = *LDSP(v4f, L_CS + w * 128 + (8 * g + 4 * hh) * 4);
    csv[4 * g] = c.x; csv[4 * g + 1] = c.y; csv[4 * g + 2] = c.z; csv[4 * g + 3] = c.w;
  }
#pragma unroll
  for (int nt = 0; nt < 4; nt++) {
    const u32 tb_ = trb + (nt >> 1) * 57344 + (((nt & 1) * 64) ^ swz);
    v16f a;
#pragma unroll
    for (int r = 0; r < 16; r++) a[r] = 0.f;
#pragma unroll
    for (int s = 0; s < 28; s++) {
      const v2u b0 = trd(tb_ + s * 2048), b1 = trd(tb_ + s * 2048 + 512);
      a = __builtin_amdgcn_mfma_f32_32x32x16_bf16(asbf(w13[s]), asbf(v4u{b0.x, b0.y, b1.x, b1.y}), a, 0, 0, 0);
    }
    float sm = 0.f, sq = 0.f;
#pragma unroll
    for (int ww = 0; ww < 8; ww++) {
      const u32 a_ = L_PART + (((nt >> 1) * 8 + ww) * 128 + (nt & 1) * 32 + (lane & 31)) * 4;
      sm += *LDSP(float, a_); sq += *LDSP(float, a_ + 256);
    }
    const float mean = sm * (1.f / D), c = fmaxf(sq * (1.f / D) - mean * mean, 0.f) + 1e-5f;
    const float k1 = -1.4426950408889634f * rsqrtf(c);
    float hv[8];
#pragma unroll
    for (int r = 0; r < 8; r++) {
      const float g = a[r] - mean * csv[r], uu = a[r + 8] - mean * csv[r + 8];
      const float e = __builtin_amdgcn_exp2f(g * k1);
      hv[r] = g * uu * __builtin_amdgcn_rcpf(e * c + c);
    }
    st128(v4u{canon(pk(hv[0], hv[4])), canon(pk(hv[1], hv[5])), canon(pk(hv[2], hv[6])), canon(pk(hv[3], hv[7]))}, rws, hfo + nt * 112 * 1024);
    SCHED();
  }
  FENCE();
  STAMP(8);
  PHASE();

  hh = lane >> 5;
  const int dt = fj % 7, q2 = fj / 7;
  const int un = w >> 2, ur = w & 3;
  v4u res;
  {
    const int nt = 2 * un + q2;
    const u32 a = trb + (nt >> 1) * 57344 + (((nt & 1) * 64) ^ swz) + (4 * dt + ur) * 2048;
    const v2u b0 = trd(a), b1 = trd(a + 512);
    res = v4u{b0.x, b0.y, b1.x, b1.y};
  }
  v16f c2[2][2];
#pragma unroll
  for (int r = 0; r < 16; r++) c2[0][0][r] = c2[0][1][r] = c2[1][0][r] = c2[1][1][r] = 0.f;
  {
    const rsrc_t rw2 = mk(gw2, F * D * 2);
    const u32 wo = (u32)((224 * w + 8 * hh) * 896 + dt * 128 + (lane & 31) * 4);
    v4u we[14], wo_[14];
#pragma unroll
    for (int s = 0; s < 14; s++) {
      u32 r[8];
#pragma unroll
      for (int j = 0; j < 8; j++) r[j] = __builtin_amdgcn_raw_buffer_load_b32(rw2, wo, (16 * s + j) * 896, 0);
      we[s] = v4u{plo(r[0], r[1]), plo(r[2], r[3]), plo(r[4], r[5]), plo(r[6], r[7])};
      wo_[s] = v4u{phi(r[0], r[1]), phi(r[2], r[3]), phi(r[4], r[5]), phi(r[6], r[7])};
    }
#pragma unroll
    for (int n = 0; n < 2; n++) {
      const u32 o = WS_HF + (u32)(((4 * rt + 2 * n + q2) * 112 + 14 * w) * 1024 + lane * 16);
      v4u hf[14];
#pragma unroll
      for (int s = 0; s < 14; s++) hf[s] = __builtin_amdgcn_raw_buffer_load_b128(rws, o, s * 1024, SC1);
      for (u32 spin = 0; spin < SPIN_MAX; spin++) {
        u32 badmask = 0;
#pragma unroll
        for (int s = 0; s < 14; s++) if (__any(max4(hf[s]) == 0xFFFFFFFFu)) badmask |= 1u << s;
        if (!badmask) break;
        FENCE();
#pragma unroll
        for (int s = 0; s < 14; s++) if (badmask >> s & 1) hf[s] = __builtin_amdgcn_raw_buffer_load_b128(rws, o, s * 1024, SC1);
      }
#pragma unroll
      for (int s = 0; s < 14; s++) {
        c2[n][0] = __builtin_amdgcn_mfma_f32_32x32x16_bf16(asbf(we[s]), asbf(hf[s]), c2[n][0], 0, 0, 0);
        c2[n][1] = __builtin_amdgcn_mfma_f32_32x32x16_bf16(asbf(wo_[s]), asbf(hf[s]), c2[n][1], 0, 0, 0);
      }
    }
  }
  BAR();
  STAMP(9);
#pragma unroll
  for (int n = 0; n < 2; n++)
#pragma unroll
    for (int g = 0; g < 4; g++)
      if (n * 4 + g != w) {
        const u32 a = (u32)(((w * 8 + n * 4 + g) * 2) * 1024 + lane * 16);
        *LDSP(v4f, a) = v4f{c2[n][0][4 * g], c2[n][0][4 * g + 1], c2[n][0][4 * g + 2], c2[n][0][4 * g + 3]};
        *LDSP(v4f, a + 1024) = v4f{c2[n][1][4 * g], c2[n][1][4 * g + 1], c2[n][1][4 * g + 2], c2[n][1][4 * g + 3]};
      }
  v4f oe, oo;
#pragma unroll
  for (int n = 0; n < 2; n++)
#pragma unroll
    for (int g = 0; g < 4; g++)
      if (n * 4 + g == w) {
        oe = v4f{c2[n][0][4 * g], c2[n][0][4 * g + 1], c2[n][0][4 * g + 2], c2[n][0][4 * g + 3]};
        oo = v4f{c2[n][1][4 * g], c2[n][1][4 * g + 1], c2[n][1][4 * g + 2], c2[n][1][4 * g + 3]};
      }
  BAR();
#pragma unroll
  for (int sq = 1; sq < 8; sq++) {
    const u32 a = (u32)(((((w + sq) & 7) * 8 + w) * 2) * 1024 + lane * 16);
    oe += *LDSP(v4f, a);
    oo += *LDSP(v4f, a + 1024);
  }
  {
    const int row = 128 * rt + 64 * un + 32 * q2 + (lane & 31);
    const v4u o = {pk(lo2f(res.x) + oe.x, hi2f(res.x) + oo.x), pk(lo2f(res.y) + oe.y, hi2f(res.y) + oo.y),
                   pk(lo2f(res.z) + oe.z, hi2f(res.z) + oo.z), pk(lo2f(res.w) + oe.w, hi2f(res.w) + oo.w)};
    *(v4u*)(gout + row * D + 64 * dt + 16 * ur + 8 * hh) = o;
  }
  if (b == 0 && tid == 0) __builtin_amdgcn_raw_buffer_store_b64(v2u{t0, t1 ^ 0x5bd1e995u}, rws, WS_EPOCH, 0, SC1);
  STAMP(10);
}

extern "C" void kernel_launch(void* const* d_in, const int* in_sizes, int n_in, void* d_out, int out_size, void* d_ws, size_t ws_size,
                              hipStream_t stream) {
  (void)hipFuncSetAttribute((const void*)fused_kernel, hipFuncAttributeMaxDynamicSharedMemorySize, L_TOTAL);
  fused_kernel<<<256, 512, L_TOTAL, stream>>>((const u16*)d_in[0], (const u16*)d_in[1], (const u16*)d_in[2], (const u16*)d_in[3],
                                              (const u16*)d_in[4], (const u16*)d_in[5], (const u16*)d_in[7], (const u16*)d_in[8],
                                              (const u16*)d_in[9], (u16*)d_out, (unsigned char*)d_ws);
}
```

```cpp
#include <hip/hip_runtime.h>
#include <hip/hip_bf16.h>
#include <type_traits>

typedef unsigned u32;
typedef unsigned short u16;
typedef unsigned long long u64;
typedef u32 v4u __attribute__((ext_vector_type(4)));
typedef u32 v2u __attribute__((ext_vector_type(2)));
typedef float v4f __attribute__((ext_vector_type(4)));
typedef float v2f __attribute__((ext_vector_type(2)));
typedef float v16f __attribute__((ext_vector_type(16)));
typedef __bf16 bf8 __attribute__((ext_vector_type(8)));
typedef __bf16 bf2 __attribute__((ext_vector_type(2)));
typedef short s4 __attribute__((ext_vector_type(4)));
typedef __amdgpu_buffer_rsrc_t rsrc_t;
#define LDSP(T, off) ((__attribute__((address_space(3))) T*)(size_t)(off))

constexpr int S = 2048, D = 448, F = 1792;
constexpr u32 WS_EPOCH = 0;
constexpr u32 WS_FLAG = 4096;
constexpr u32 WS_FLAGB = 8192;
constexpr u32 WS_FLAGC = 12288;
constexpr u32 WS_CS = 16384;
constexpr u32 WS_KVG = 32768;
constexpr u32 WS_XQ = WS_KVG + 256u * 224 * 64;
constexpr u32 WS_HT = WS_XQ + 256u * 224 * 64;
constexpr u32 WS_HF = WS_HT + 32u * 448 * 128;
constexpr u32 WS_END = WS_HF + 128u * 56 * 1024;
constexpr u32 WS_DBG = WS_END;
constexpr u32 L_W = 0;
constexpr u32 L_TAP = 114688;
constexpr u32 L_RX = 147968;
constexpr u32 L_DUM = 157184;
constexpr u32 L_STAT = 162816, L_CNT = L_STAT + 64;
constexpr u32 L_RED = 114688;
constexpr u32 L_Q0 = 114688, L_Q1 = 4096;
constexpr u32 L_ST = 143360;
constexpr u32 L_CSV = 145408;
constexpr u32 L_TOTAL = 160 * 1024;
constexpr u32 OOB = 0x80000000u;
constexpr int SC1 = 16;
constexpr u32 SPIN_MAX = 1u << 16;

#define FENCE() asm volatile("" ::: "memory")
#define WAITV(n) asm volatile("s_waitcnt vmcnt(" #n ")" ::: "memory")
#define DRAINV() __builtin_amdgcn_s_waitcnt(0x0F70)
#define BAR() do { asm volatile("s_waitcnt lgkmcnt(0)" ::: "memory"); __builtin_amdgcn_s_barrier(); FENCE(); } while (0)
#define SCHED() __builtin_amdgcn_sched_barrier(0)
#define PHASE() do { SCHED(); asm volatile("" : "+v"(tid)); lane = tid & 63; SCHED(); } while (0)
#define UNLIKELY(x) __builtin_expect(!!(x), 0)
#define USE(x) asm volatile("" ::"v"(x))
#define OPQ(x) asm volatile("" : "+v"(x))

__device__ __forceinline__ rsrc_t mk(const void* p, u32 bytes) { return __builtin_amdgcn_make_buffer_rsrc((void*)p, 0, bytes, 0x00020000); }
__device__ __forceinline__ v4u mkv(const void* p, u32 bytes) { const u64 a = (u64)p; return v4u{(u32)a, (u32)(a >> 32) & 0xffffu, bytes, 0x00020000u}; }
__device__ __forceinline__ float lo2f(u32 v) { return __builtin_bit_cast(float, v << 16); }
__device__ __forceinline__ float hi2f(u32 v) { return __builtin_bit_cast(float, v & 0xffff0000u); }
__device__ __forceinline__ u32 pk(float a, float b) { return __builtin_bit_cast(u32, __builtin_convertvector(v2f{a, b}, bf2)); }
__device__ __forceinline__ u32 plo(u32 a, u32 b) { return __builtin_amdgcn_perm(b, a, 0x05040100u); }
__device__ __forceinline__ u32 phi(u32 a, u32 b) { return __builtin_amdgcn_perm(b, a, 0x07060302u); }
__device__ __forceinline__ u32 canon(u32 v) { return min(v, 0xFFFFFFFEu); }
__device__ __forceinline__ u32 max4(v4u t) { return max(max(t.x, t.y), max(t.z, t.w)); }
__device__ __forceinline__ u32 max5(u32 m, v4u t) { return max(max(max(m, t.x), t.y), max(t.z, t.w)); }
__device__ __forceinline__ bf8 asbf(v4u v) { return __builtin_bit_cast(bf8, v); }
__device__ __forceinline__ v2u trd(u32 a) { return __builtin_bit_cast(v2u, __builtin_amdgcn_ds_read_tr16_b64_v4i16(LDSP(s4, a))); }
template <int O> __device__ __forceinline__ v4u trd2(u32 a) { const v2u x = trd(a), y = trd(a + O); return v4u{x.x, x.y, y.x, y.y}; }
__device__ __forceinline__ void st128(v4u v, rsrc_t r, u32 off) { __builtin_amdgcn_raw_buffer_store_b128(v, r, off, 0, SC1); }
__device__ __forceinline__ void st64(u32 a, u32 b_, rsrc_t r, u32 off) { __builtin_amdgcn_raw_buffer_store_b64(v2u{a, b_}, r, off, 0, SC1); }
__device__ __forceinline__ v4u ld128(rsrc_t r, u32 off, u32 so) { return __builtin_amdgcn_raw_buffer_load_b128(r, off, so, SC1); }
__device__ __forceinline__ v2u ld64(rsrc_t r, u32 off, u32 so) { return __builtin_amdgcn_raw_buffer_load_b64(r, off, so, SC1); }
__device__ __forceinline__ u32 f2u(float v) { return __builtin_bit_cast(u32, v); }
__device__ __forceinline__ float u2f(u32 v) { return __builtin_bit_cast(float, v); }
__device__ __forceinline__ float opq(float v) { asm("" : "+v"(v)); return v; }
template <int CTRL> __device__ __forceinline__ float dppadd(float v) { return v + u2f(__builtin_amdgcn_update_dpp(0, f2u(v), CTRL, 0xF, 0xF, true)); }
__device__ __forceinline__ float swap16add(float a, float b) { const auto r = __builtin_amdgcn_permlane16_swap(f2u(a), f2u(b), false, false); return u2f(r[0]) + u2f(r[1]); }
__device__ __forceinline__ float swap32add(float a, float b) { const auto r = __builtin_amdgcn_permlane32_swap(f2u(a), f2u(b), false, false); return u2f(r[0]) + u2f(r[1]); }
__device__ __forceinline__ float wsum(float v) {
  v = dppadd<0xB1>(v); v = dppadd<0x4E>(v); v = dppadd<0x141>(v); v = dppadd<0x140>(v);
  v = swap16add(v, v);
  return swap32add(v, v);
}
template <u32 LI, u32 SO> __device__ __forceinline__ void dma16(u32 lb, u32 vo, v4u r) {
  u32 t;
  asm volatile("s_add_u32 m0, %1, %4\n\ts_mov_b32 %0, %5\n\tbuffer_load_dwordx4 %2, %3, %0 offen lds"
               : "=&s"(t) : "s"(lb), "v"(vo), "s"(r), "n"(LI), "n"(SO) : "memory", "scc");
}
__device__ __forceinline__ void warm(u32 vo, v4u r) {
  asm volatile("s_mov_b32 m0, %2\n\ts_nop 0\n\tbuffer_load_dword %0, %1, 0 offen lds" ::"v"(vo), "s"(r), "n"(L_DUM) : "memory");
}
template <int S0, int S1, int MS> __device__ __forceinline__ void dmaw(u32 lb, u32 vo, v4u r) {
  if constexpr (S0 < S1) { dma16<(S0 % 14) * 8192u + MS * 4096u, S0 * 57344u>(lb, vo, r); dmaw<S0 + 1, S1, MS>(lb, vo, r); }
}
template <int S0, int S1> __device__ __forceinline__ void dmaw2(u32 lb, u32 vo1, u32 vo3, v4u r1, v4u r3) {
  if constexpr (S0 < S1) { dma16<S0 * 8192u, S0 * 57344u>(lb, vo1, r1); dma16<S0 * 8192u + 4096u, S0 * 57344u>(lb, vo3, r3); dmaw2<S0 + 1, S1>(lb, vo1, vo3, r1, r3); }
}
#define MFMA32(a, b, c) __builtin_amdgcn_mfma_f32_32x32x16_bf16(asbf(a), asbf(b), c, 0, 0, 0)
#define MFMA16(a, b, c) __builtin_amdgcn_mfma_f32_16x16x32_bf16(asbf(a), asbf(b), c, 0, 0, 0)
#define STAMP(i)
#define STOUT()
#define DLY1 0
#define DLY2 0
#define NE 5
#define NW2E 2

template <int N, class SOF> __device__ __forceinline__ void settle(v4u* t, rsrc_t r, u32 off, SOF so) {
  u32 m = 0;
#pragma unroll
  for (int i = 0; i < N; i++) m = max5(m, t[i]);
  if (UNLIKELY(__any(m == 0xFFFFFFFFu))) {
#pragma nounroll
    for (u32 spin = 0; spin < SPIN_MAX; spin++) {
      bool any = false;
      FENCE();
#pragma unroll
      for (int i = 0; i < N; i++)
        if (__any(max4(t[i]) == 0xFFFFFFFFu)) { t[i] = ld128(r, off, so(i)); any = true; }
      if (!any) break;
    }
    DRAINV();
  }
}
__device__ __forceinline__ void waitflag(v2u f, rsrc_t r, u32 off, u32 t0, u32 t1) {
  if (UNLIKELY(__any((f.x != t0) | (f.y != t1)))) {
#pragma nounroll
    for (u32 spin = 0; spin < SPIN_MAX; spin++) {
      FENCE();
      f = ld64(r, off, 0);
      if (!__any((f.x != t0) | (f.y != t1))) break;
    }
    DRAINV();
  }
  FENCE();
}

__global__ void __launch_bounds__(512, 2)
fused_kernel(const u16* __restrict__ gx, const u16* __restrict__ gk, const u16* __restrict__ gv, const u16* __restrict__ gsk,
             const u16* __restrict__ gssm, const u16* __restrict__ gsd, const u16* __restrict__ gw1, const u16* __restrict__ gw3,
             const u16* __restrict__ gw2, u16* __restrict__ gout, unsigned char* __restrict__ ws) {
  asm volatile("" ::"s"(gx), "s"(gk), "s"(gv), "s"(gsk), "s"(gssm), "s"(gsd), "s"(gw1), "s"(gw3), "s"(gw2), "s"(gout), "s"(ws));
  int tid = threadIdx.x, lane = tid & 63;
  const int w = __builtin_amdgcn_readfirstlane(tid >> 6), b = blockIdx.x;
  const int xc = b & 7, ii = b >> 3;
  const bool mainwg = ii < 28;
  const int p = xc * 28 + ii;
  const int dt = ii % 7, q2 = xc & 1;
  const int rt = 4 * (xc >> 1) + ii / 7, fj = 7 * q2 + dt;
  const rsrc_t rws = mk(ws, WS_END);
  const v4u poison = {0xFFFFFFFFu, 0xFFFFFFFFu, 0xFFFFFFFFu, 0xFFFFFFFFu};
  STAMP(0);

  u32 t0, t1;
  {
    const int pp = tid & 255;
    const u32 colo = pp < 224 ? (u32)(8 * b * 896 + pp * 4) : OOB;
    auto kvpart = [&](auto mw) {
      constexpr bool MW = decltype(mw)::value;
      u32 ra[11], rb[8];
      const rsrc_t rk = mk(gk, S * D * 2), rv = mk(gv, S * D * 2);
      const u32 halo = (b > 0 && pp < 224) ? colo - 3 * 896 : OOB;
#pragma unroll
      for (int i = 0; i < 3; i++) ra[i] = __builtin_amdgcn_raw_buffer_load_b32(rk, halo + i * 896, 0, 0);
#pragma unroll
      for (int i = 0; i < 8; i++) ra[3 + i] = __builtin_amdgcn_raw_buffer_load_b32(rk, colo + (i & 3) * 896, (i >> 2) * 3584, 0);
      const v4u skw = __builtin_amdgcn_raw_buffer_load_b128(mk(gsk, D * 8), pp * 16, 0, 0);
#pragma unroll
      for (int i = 0; i < 8; i++) rb[i] = __builtin_amdgcn_raw_buffer_load_b32(rv, colo + (i & 3) * 896, (i >> 2) * 3584, 0);
      const v2u ep = ld64(rws, WS_EPOCH, 0);
      FENCE();
      USE(skw);
      STAMP(1);
      v4u tp[4], cs[28];
      if constexpr (MW) {
        const rsrc_t rt_ = mk(gssm, D * S * 2);
        const u32 o = (u32)(2 * p * 4096 + pp * 16);
#pragma unroll
        for (int c = 0; c < 2; c++) {
          tp[2 * c] = __builtin_amdgcn_raw_buffer_load_b128(rt_, pp > 0 ? o - 16 : OOB, c * 4096, 0);
          tp[2 * c + 1] = __builtin_amdgcn_raw_buffer_load_b128(rt_, o, c * 4096, 0);
        }
      }
      FENCE();
      const float s0[4] = {lo2f(skw.x), hi2f(skw.x), lo2f(skw.y), hi2f(skw.y)};
      const float s1[4] = {lo2f(skw.z), hi2f(skw.z), lo2f(skw.w), hi2f(skw.w)};
      float k0[8], k1[8];
#pragma unroll
      for (int i = 0; i < 8; i++) {
        float a0 = lo2f(ra[i + 3]) * s0[0], a1 = hi2f(ra[i + 3]) * s1[0];
#pragma unroll
        for (int j = 1; j < 4; j++) { a0 += lo2f(ra[i + 3 - j]) * s0[j]; a1 += hi2f(ra[i + 3 - j]) * s1[j]; }
        k0[i] = a0 * lo2f(rb[i]); k1[i] = a1 * hi2f(rb[i]);
      }
      *LDSP(v4u, L_RX + pp * 32) = v4u{pk(k0[3], k0[2]), pk(k0[1], k0[0]), pk(k0[7], k0[6]), pk(k0[5], k0[4])};
      *LDSP(v4u, L_RX + pp * 32 + 16) = v4u{pk(k1[3], k1[2]), pk(k1[1], k1[0]), pk(k1[7], k1[6]), pk(k1[5], k1[4])};
      t0 = __builtin_amdgcn_readfirstlane(ep.x) + 1u; t1 = __builtin_amdgcn_readfirstlane(ep.y) ^ 0x5bd1e995u;
      FENCE();
      v2u pl[4];
#pragma unroll
      for (int i = 0; i < 4; i++) pl[i] = *LDSP(v2u, L_RX + w * 2048 + lane * 8 + i * 512);
      const u32 o = WS_KVG + b * 14336 + w * 4096 + lane * 16;
#pragma unroll
      for (int i = 0; i < 4; i++)
        if (i < 2 || w < 3) st128(v4u{pl[i].x, pl[i].y, t0, t1}, rws, o + i * 1024);
      FENCE();
      STAMP(2);
      BAR();
      STAMP(3);
      if constexpr (MW) {
#pragma unroll
        for (int i = 0; i < 4; i++) OPQ(tp[i]);
#pragma unroll
        for (int c = 0; c < 2; c++) {
          const u32 a[8] = {tp[2 * c].x, tp[2 * c].y, tp[2 * c].z, tp[2 * c].w, tp[2 * c + 1].x, tp[2 * c + 1].y, tp[2 * c + 1].z, tp[2 * c + 1].w};
          u32 od[7];
#pragma unroll
          for (int j = 0; j < 7; j++) od[j] = __builtin_amdgcn_alignbit(a[j + 1], a[j], 16);
          const u32 base = L_TAP + c * 16640 + (pp + 1) * 16;
          *LDSP(v4u, base) = v4u{od[0], od[1], od[2], od[3]};
          *LDSP(v4u, base + 4160) = v4u{a[1], a[2], a[3], a[4]};
          *LDSP(v4u, base + 8320) = v4u{od[1], od[2], od[3], od[4]};
          *LDSP(v4u, base + 12480) = v4u{a[2], a[3], a[4], a[5]};
          if (pp == 255) {
            *LDSP(v2u, base + 16) = v2u{od[4], od[5]};
            *LDSP(v2u, base + 16 + 4160) = v2u{a[5], a[6]};
            *LDSP(v2u, base + 16 + 8320) = v2u{od[5], od[6]};
            *LDSP(v2u, base + 16 + 12480) = v2u{a[6], a[7]};
          }
        }
        if (tid < 8) *LDSP(v4u, L_TAP + tid * 4160) = v4u{0u, 0u, 0u, 0u};
      } else {
        const int j = b - 224;
        const rsrc_t rw = mk(j < 16 ? gw1 : gw3, D * F * 2);
        const u32 o = pp < 224 ? (u32)((pp / 14) * 28 * 3584 + (j & 15) * 224 + (pp % 14) * 16) : OOB;
#pragma unroll
        for (int i = 0; i < 28; i++) cs[i] = __builtin_amdgcn_raw_buffer_load_b128(rw, o, i * 3584, 0);
        float sm[8];
#pragma unroll
        for (int e = 0; e < 8; e++) sm[e] = 0.f;
#pragma unroll
        for (int i = 0; i < 28; i++)
#pragma unroll
          for (int e = 0; e < 4; e++) { sm[2 * e] += lo2f(cs[i][e]); sm[2 * e + 1] += hi2f(cs[i][e]); }
        *LDSP(v4f, pp * 32) = v4f{sm[0], sm[1], sm[2], sm[3]};
        *LDSP(v4f, pp * 32 + 16) = v4f{sm[4], sm[5], sm[6], sm[7]};
      }
    };
    if (w < 4) {
      if (mainwg) kvpart(std::true_type{}); else kvpart(std::false_type{});
    } else {
      const rsrc_t rx = mk(gx, S * D * 2);
      const u32 so = lane < 56 ? (u32)((8 * b + 2 * (w - 4)) * 896 + lane * 16) : OOB;
      v4u sr[2];
      sr[0] = __builtin_amdgcn_raw_buffer_load_b128(rx, so, 0, 0);
      sr[1] = __builtin_amdgcn_raw_buffer_load_b128(rx, so + 896, 0, 0);
      u32 rb[8];
#pragma unroll
      for (int i = 0; i < 8; i++) rb[i] = __builtin_amdgcn_raw_buffer_load_b32(rx, colo + (i & 3) * 896, (i >> 2) * 3584, 0);
      const v2u ep = ld64(rws, WS_EPOCH, 0);
      FENCE();
      {
        const int tt = 512 * (w - 4) + 256 * ((lane >> 4) & 1) + 16 * (lane & 15) + 4 * ((lane >> 4) & 2);
        const u32 o = mainwg ? WS_HT + (tt >> 6) * 57344 + 2 * p * 128 + (tt & 63) * 2 : OOB;
        st128(poison, rws, o);
        st128(poison, rws, o + 128);
      }
      FENCE();
      const v4u r1 = mkv(gw1, D * F * 2), r3 = mkv(gw3, D * F * 2);
      const u32 wb = L_W + (w - 4) * 1024;
      const u32 wrow = (u32)((4 * (w - 4) + (lane >> 4)) * 3584 + fj * 256);
      const u32 vo1 = wrow + (((lane & 15) ^ (2 * (lane >> 4))) * 16), vo3 = vo1 ^ 128;
      USE(ep); USE(rb[7]);
      STAMP(1);
      {
        const u32 o = (u32)((28 * (ii & 15) + pp / 14) * 3584 + q2 * 1792 + (pp % 14) * 128);
        const v4u rr = ii < 16 ? r1 : r3;
        warm(pp < 252 ? o : OOB, rr);
        warm(pp < 140 ? o + 18 * 3584 : OOB, rr);
      }
      if (mainwg) dmaw2<0, NE>(wb, vo1, vo3, r1, r3);
      float mean[2], rstd[2];
#pragma unroll
      for (int r = 0; r < 2; r++) {
        const float e[8] = {lo2f(sr[r].x), hi2f(sr[r].x), lo2f(sr[r].y), hi2f(sr[r].y), lo2f(sr[r].z), hi2f(sr[r].z), lo2f(sr[r].w), hi2f(sr[r].w)};
        const float s = ((e[0] + e[1]) + (e[2] + e[3])) + ((e[4] + e[5]) + (e[6] + e[7]));
        mean[r] = wsum(s) * (1.f / D);
        float q = 0.f;
#pragma unroll
        for (int i = 0; i < 8; i++) { const float d = e[i] - mean[r]; q += d * d; }
        q = wsum(lane < 56 ? q : 0.f);
        rstd[r] = __builtin_amdgcn_rsqf(q * (1.f / D) + 1e-5f);
      }
      if (lane == 0) *LDSP(v4f, L_STAT + (w - 4) * 16) = v4f{mean[0], rstd[0], mean[1], rstd[1]};
      if (tid == 256) *LDSP(u32, L_CNT) = 0u;
      t0 = __builtin_amdgcn_readfirstlane(ep.x) + 1u; t1 = __builtin_amdgcn_readfirstlane(ep.y) ^ 0x5bd1e995u;
      STAMP(2);
      BAR();
      STAMP(3);
      v4f sa[4];
#pragma unroll
      for (int i = 0; i < 4; i++) sa[i] = *LDSP(v4f, L_STAT + i * 16);
      const u32 xb = L_W + 12 * 8192 + (w - 4) * 1024;
#pragma unroll
      for (int h = 0; h < 2; h++) {
        const v4f a = sa[2 * h], c = sa[2 * h + 1];
        const u32 *x = rb + 4 * h;
        *LDSP(v4u, xb + (lane >> 4) * 4096 + (lane & 15) * 64 + h * 16) =
            v4u{plo(x[0], x[1]), plo(x[2], x[3]), pk((lo2f(x[0]) - a.x) * a.y, (lo2f(x[1]) - a.z) * a.w), pk((lo2f(x[2]) - c.x) * c.y, (lo2f(x[3]) - c.z) * c.w)};
        *LDSP(v4u, xb + (lane >> 4) * 4096 + (lane & 15) * 64 + 32 + h * 16) =
            v4u{phi(x[0], x[1]), phi(x[2], x[3]), pk((hi2f(x[0]) - a.x) * a.y, (hi2f(x[1]) - a.z) * a.w), pk((hi2f(x[2]) - c.x) * c.y, (hi2f(x[3]) - c.z) * c.w)};
      }
      FENCE();
      v4u px[4];
#pragma unroll
      for (int i = 0; i < 4; i++) px[i] = *LDSP(v4u, xb + i * 4096 + lane * 16);
      const u32 o = WS_XQ + b * 14336 + (w - 4) * 4096 + lane * 16;
#pragma unroll
      for (int i = 0; i < 4; i++)
        if (i < 2 || w < 7) st128(px[i], rws, o + i * 1024);
      WAITV(0);
      STAMP(4);
      if (lane == 0) {
        const u32 old = __hip_atomic_fetch_add(LDSP(u32, L_CNT), 1u, __ATOMIC_RELAXED, __HIP_MEMORY_SCOPE_WORKGROUP);
        if (old == 3u) st64(t0, t1, rws, WS_FLAG + b * 8);
      }
      FENCE();
      if (mainwg) dmaw2<NE, 14>(wb, vo1, vo3, r1, r3);
      STAMP(5);
    }
  }
  PHASE();
  if (!mainwg) {
    BAR();
    if (w == 0) {
      v4f s = {0.f, 0.f, 0.f, 0.f};
#pragma unroll
      for (int g = 0; g < 16; g++) s += *LDSP(v4f, g * 448 + lane * 16);
      st128(__builtin_bit_cast(v4u, s), rws, lane < 28 ? WS_CS + (b - 224) * 448 + lane * 16 : OOB);
      WAITV(0);
      if (lane == 0) st64(t0, t1, rws, WS_FLAGC + (b - 224) * 8);
    }
    STAMP(23);
    STOUT();
    return;
  }

  if (w < 4) {
    STAMP(4);
    if (DLY1) __builtin_amdgcn_s_sleep(DLY1);
    v4u g[4];
    const u32 o = WS_KVG + (tid >> 2) * 14336 + p * 64 + (tid & 3) * 16;
#pragma nounroll
    for (u32 spin = 0; spin < SPIN_MAX; spin++) {
      FENCE();
      bool bad = false;
#pragma unroll
      for (int i = 0; i < 4; i++) g[i] = ld128(rws, o, i * 64 * 14336);
#pragma unroll
      for (int i = 0; i < 4; i++) bad |= (g[i].z != t0) | (g[i].w != t1);
      if (!__any(bad)) break;
      __builtin_amdgcn_s_sleep(2);
    }
    STAMP(5);
    const u32 la = L_RX + ((tid >> 1) & 1) * 4608 + (508 - 8 * (tid >> 2) - 4 * (tid & 1)) * 2;
#pragma unroll
    for (int i = 0; i < 4; i++) *LDSP(v2u, la + (3 - i) * 1024) = v2u{g[i].x, g[i].y};
    if (tid < 64) *LDSP(v4u, L_RX + (tid >> 5) * 4608 + 4096 + (tid & 31) * 16) = v4u{0u, 0u, 0u, 0u};
  }
  const int ch = w >> 2, wq = w & 3;
  const float dval = lo2f(gsd[2 * p + ch]);
  const u32 fo1 = WS_FLAG + (64 * wq + lane) * 8;
  BAR();
  STAMP(6);
  v4u w13[28];
  const u32 wra = L_W + (((lane >> 4) & 1) * 4 + 2 * (lane >> 5)) * 1024 + ((lane & 15) >> 2) * 256 +
                  (((2 * w + (lane & 1)) ^ (2 * ((lane & 15) >> 2)) ^ (8 * ((lane >> 4) & 1))) * 16) + ((lane >> 1) & 1) * 8;
  {
    const v2u f1 = ld64(rws, fo1, 0);
    FENCE();
    const int la_ = lane & 15, kg = lane >> 4;
    v4f acc[8];
#pragma unroll
    for (int n = 0; n < 8; n++) acc[n] = v4f{0.f, 0.f, 0.f, 0.f};
    u32 aa = L_TAP + ch * 16640 + (la_ & 3) * 4160 + ((la_ >> 2) + 2 * kg + 16 * wq) * 8, ab = aa + 8;
    asm volatile("" : "+v"(ab));
    const u32 ba = L_RX + ch * 4608 + (30 + 8 * wq - 2 * la_ + kg) * 16;
    auto lda = [&](int o) { const v2u x = *LDSP(v2u, aa + o), y = *LDSP(v2u, ab + o); return v4u{x.x, x.y, y.x, y.y}; };
    v4u A[8], B[8], A2[8], B2[8];
#pragma unroll
    for (int m = 0; m < 8; m++) { A[m] = lda(m * 512); B[m] = *LDSP(v4u, ba + (7 - m) * 512); SCHED(); }
#pragma unroll
    for (int g = 0; g < 8; g++) {
      A2[g] = lda(64 + g * 512); B2[g] = *LDSP(v4u, ba + 64 + (7 - g) * 512);
#pragma unroll
      for (int m = 0; m <= g; m++) {
        if (m + g < 8) acc[m + g] = MFMA16(A[m], B[g], acc[m + g]);
        if (m < g && m + g < 8) acc[m + g] = MFMA16(A[g], B[m], acc[m + g]);
      }
      SCHED();
    }
    const int t = 512 * wq + 16 * la_ + 4 * kg;
    const v2u kv0 = *LDSP(v2u, L_RX + ch * 4608 + (2044 - t) * 2), kv1 = *LDSP(v2u, L_RX + ch * 4608 + (2044 - 256 - t) * 2);
    waitflag(f1, rws, fo1, t0, t1);
    WAITV(0);
    STAMP(7);
    BAR();
    STAMP(8);
    const u32 xqo = WS_XQ + (64 * wq + 2 * la_ + (kg >> 1)) * 14336 + p * 64 + ch * 32 + (kg & 1) * 16;
    const v4u xq0 = ld128(rws, xqo, 0), xq1 = ld128(rws, xqo, 32 * 14336);
    FENCE();
#pragma unroll
    for (int d = 7; d >= 0; d--) {
      if (d > 0) { w13[14 - 2 * d] = trd2<1024>(wra + (14 - 2 * d) * 8192); w13[15 - 2 * d] = trd2<1024>(wra + (15 - 2 * d) * 8192); }
#pragma unroll
      for (int m = 0; m <= d; m++) acc[d] = MFMA16(A2[m], B2[d - m], acc[d]);
      SCHED();
    }
#pragma unroll
    for (int n = 7; n >= 0; n--)
      if ((n >> 1) != wq) *LDSP(v4f, L_RED + (w * 6 + (n < 2 * wq ? n : n - 2)) * 1024 + lane * 16) = acc[n];
    v4f y0, y1;
#pragma unroll
    for (int n = 0; n < 4; n++) if (n == wq) { y0 = acc[2 * n]; y1 = acc[2 * n + 1]; }
    BAR();
    STAMP(9);
#pragma unroll
    for (int sq = 1; sq < 4; sq++) {
      const int sw = (wq + sq) & 3;
      const u32 a = L_RED + ((ch * 4 + sw) * 6 + (wq < sw ? 2 * wq : 2 * wq - 2)) * 1024 + lane * 16;
      y0 += *LDSP(v4f, a);
      y1 += *LDSP(v4f, a + 1024);
    }
    const u32 h0x = pk(lo2f(xq0.x) + lo2f(xq0.z) * (y0.x + hi2f(kv0.y) * dval), hi2f(xq0.x) + hi2f(xq0.z) * (y0.y + lo2f(kv0.y) * dval));
    const u32 h0y = pk(lo2f(xq0.y) + lo2f(xq0.w) * (y0.z + hi2f(kv0.x) * dval), hi2f(xq0.y) + hi2f(xq0.w) * (y0.w + lo2f(kv0.x) * dval));
    const u32 h1x = pk(lo2f(xq1.x) + lo2f(xq1.z) * (y1.x + hi2f(kv1.y) * dval), hi2f(xq1.x) + hi2f(xq1.z) * (y1.y + lo2f(kv1.y) * dval));
    const u32 h1y = pk(lo2f(xq1.y) + lo2f(xq1.w) * (y1.z + hi2f(kv1.x) * dval), hi2f(xq1.y) + hi2f(xq1.w) * (y1.w + lo2f(kv1.x) * dval));
    const auto sx = __builtin_amdgcn_permlane16_swap(canon(h0x), canon(h1x), false, false);
    const auto sy = __builtin_amdgcn_permlane16_swap(canon(h0y), canon(h1y), false, false);
    const int tt = 512 * wq + 256 * (kg & 1) + 16 * la_ + 4 * (kg & 2);
    st128(v4u{sx[0], sy[0], sx[1], sy[1]}, rws, WS_HT + (tt >> 6) * 57344 + (2 * p + ch) * 128 + (tt & 63) * 2);
  }
  FENCE();
  STAMP(10);
  PHASE();

  {
    const v4u rw = mkv(w < 4 ? gw1 : gw3, D * F * 2);
    const u32 vo = (u32)((4 * wq + (lane >> 4)) * 3584 + fj * 256) + (((lane & 15) ^ (2 * (lane >> 4)) ^ (8 * ch)) * 16);
    dmaw<14, 28, 0>(L_W + w * 1024, vo, rw);
  }
  int hh = lane >> 5;
  u32 hfo = WS_HF + (u32)(((rt * 2 + ((lane >> 4) & 1)) * 4 * 56 + 4 * fj + (w >> 1)) * 1024 + ((2 * (w & 1) + hh) * 16 + (lane & 15)) * 16);
#pragma unroll
  for (int i = 0; i < 4; i++) st128(poison, rws, hfo + i * 56 * 1024);
  OPQ(hfo);
  FENCE();
  v4u csl;
  if (w == 7) {
    const u32 fo = WS_FLAGC + (lane & 31) * 8;
    waitflag(ld64(rws, fo, 0), rws, fo, t0, t1);
    csl = ld128(rws, WS_CS + hh * 7168 + fj * 512 + (lane & 31) * 16, 0);
  }
  if (DLY2) __builtin_amdgcn_s_sleep(DLY2);
  v4u hc[14];
  const u32 hto = WS_HT + rt * 114688 - 8192 + (tid >> 2) * 128 + (tid & 3) * 16;
  const u32 so3 = w < 4 ? 57344u : 64u;
  auto hso = [&](int i) -> u32 { const int j = i % 7; return (i / 7) * 57344u + (j < 3 ? 8192u + j * 16384u : j == 3 ? so3 : 64u + 16384u * (j - 3)); };
#pragma unroll
  for (int i = 0; i < 4; i++) hc[i] = ld128(rws, hto, hso(i));
  FENCE();
  settle<4>(hc, rws, hto, hso);
  WAITV(0);
#pragma unroll
  for (int i = 4; i < 14; i++) hc[i] = ld128(rws, hto, hso(i));
  FENCE();
  STAMP(11);
  BAR();
#pragma unroll
  for (int s = 14; s < 28; s++) w13[s] = trd2<1024>(wra + (s - 14) * 8192);
#pragma unroll
  for (int i = 0; i < 3; i++) *LDSP(v4u, L_Q0 + tid * 16 + i * 8192) = hc[i];
  if (w < 4) *LDSP(v4u, L_Q0 + tid * 16 + 24576) = hc[3];
  if (w == 7) *LDSP(v4u, L_CSV + lane * 16) = csl;
  BAR();
  STAMP(12);
  if (tid == 0) st64(t0, t1, rws, WS_FLAGB + b * 8);
  warm(tid < 448 ? (u32)((64 * ii + tid / 7) * 896 + (tid % 7) * 128) : OOB, mkv(gw2, F * D * 2));
  const v4u ones = {0x3F803F80u, 0x3F803F80u, 0x3F803F80u, 0x3F803F80u};
  const u32 trb = (8 * hh + ((lane & 15) >> 2)) * 64 + ((lane >> 4) & 1) * 32 + (lane & 3) * 8;
  const u32 lw = L_Q1 - 4096 + tid * 16;
  auto stats = [&](auto ns, int rg, int s0, int slot) {
    constexpr int NS = decltype(ns)::value;
    const u32 sb = (rg < 2 ? L_Q0 : L_Q1 + ((rg >> 1) - 1) * 28672) + s0 * 2048 + (8 * (lane >> 4) + ((lane & 15) >> 2)) * 64 + (rg & 1) * 32 + (lane & 3) * 8;
    v4f g = {0.f, 0.f, 0.f, 0.f}, sm = {0.f, 0.f, 0.f, 0.f};
    v4u fr[NS];
#pragma unroll
    for (int s = 0; s < NS; s++) fr[s] = trd2<256>(sb + s * 2048);
#pragma unroll
    for (int s = 0; s < NS; s++) { g = MFMA16(fr[s], fr[s], g); sm = MFMA16(ones, fr[s], sm); }
    const int i = lane & 3;
    const float sq = i == 0 ? g.x : i == 1 ? g.y : i == 2 ? g.z : g.w;
    if (((lane & 15) >> 2) == (lane >> 4)) {
      const u32 a = L_ST + (16 * rg + (lane & 15)) * 16;
      if (NS == 14) *LDSP(v4f, a) = v4f{sm.x, sq, 0.f, 0.f}; else *LDSP(v2f, a + slot * 8) = v2f{sm.x, sq};
    }
  };
  float nm, ny, cv;
  auto getst = [&](int nt) {
    const v4f s = *LDSP(v4f, L_ST + (32 * nt + (lane & 31)) * 16);
    nm = opq(s.x + s.z) * (-1.f / D);
    cv = fmaxf(__builtin_fmaf(-nm, nm, opq(s.y + s.w) * (1.f / D)), 0.f) + 1e-5f;
    ny = -1.4426950408889634f * __builtin_amdgcn_rsqf(cv);
  };
  auto epi = [&](float a, float u, float c1, float c3) -> float {
    const float g = opq(__builtin_fmaf(nm, c1, a)), uu = opq(__builtin_fmaf(nm, c3, u));
    const float e = __builtin_amdgcn_exp2f(opq(g * ny));
    return opq(opq(g * uu) * __builtin_amdgcn_rcpf(opq(__builtin_fmaf(e, cv, cv))));
  };
  if (w < 4) stats(std::integral_constant<int, 7>{}, w >> 1, 7 * (w & 1), w & 1);
  SCHED();
  const rsrc_t rw2 = mk(gw2, F * D * 2);
  u32 w2o = (u32)((224 * w + 8 * (lane >> 4)) * 896 + dt * 128 + (lane & 15) * 8);
  OPQ(w2o);
  const u32 h2o = WS_HF + (u32)(((rt * 2 + q2) * 4 * 56 + 7 * w) * 1024 + lane * 16);
  const u32 csa = L_CSV + (16 * w + 8 * hh) * 4;
  auto lin = [](int i) -> u32 { return i * 1024u; };
  v2u w2r[56], fb;
  v4u hf[4][7], res;
  v16f ac[4];
  constexpr int PD = 4;
#pragma unroll
  for (int nt = 0; nt < 4; nt++) {
    u32 tb_ = trb + (nt == 0 ? L_Q0 : L_Q1 + (nt - 1) * 28672);
    OPQ(tb_);
    v4u bfr[28];
#pragma unroll
    for (int s = 0; s < PD; s++) bfr[s] = trd2<256>(tb_ + s * 1024);
    float hv[8];
    v4f c1, c3;
    if (nt > 0) getst(nt - 1);
#pragma unroll
    for (int r = 0; r < 16; r++) ac[nt][r] = 0.f;
    u32 fbo;
    if (nt == 2) {
      fbo = WS_FLAGB + (u32)(((7 * (rt & 3) + min(lane, 13) % 7) * 8 + (xc & 6) + min(lane, 13) / 7) * 8);
      fb = ld64(rws, fbo, 0);
    }
    SCHED();
#pragma unroll
    for (int s = 0; s < 28; s++) {
      if (s + PD < 28) bfr[s + PD] = trd2<256>(tb_ + (s + PD) * 1024);
      if (nt > 0 && (s == 0 || s == 9)) { c1 = *LDSP(v4f, csa + (s ? 16 : 0)); c3 = *LDSP(v4f, csa + 512 + (s ? 16 : 0)); }
      ac[nt] = MFMA32(w13[s], bfr[s], ac[nt]);
      if (nt == 0) {
        if (s == 8) settle<3>(hc + 4, rws, hto, [&](int i) { return hso(i + 4); });
        if (s == 7 && w >= 4) *LDSP(v4u, lw) = hc[3];
        if (s >= 8 && s < 11) *LDSP(v4u, lw + (s - 7) * 8192) = hc[s - 4];
        if (s == 15) settle<4>(hc + 7, rws, hto, [&](int i) { return hso(i + 7); });
        if (s >= 15 && s < 19) *LDSP(v4u, lw + (s - 11) * 8192) = hc[s - 8];
        if (s == 22) settle<3>(hc + 11, rws, hto, [&](int i) { return hso(i + 11); });
        if (s >= 22 && s < 25) *LDSP(v4u, lw + (s - 14) * 8192) = hc[s - 11];
      } else {
        if (s >= 2 && s < 18 && (s & 1) == 0) { const int r = (s - 2) >> 1; hv[r] = epi(ac[nt - 1][r], ac[nt - 1][r + 8], c1[r & 3], c3[r & 3]); }
        if (s == 18) st128(v4u{canon(pk(hv[0], hv[1])), canon(pk(hv[2], hv[3])), canon(pk(hv[4], hv[5])), canon(pk(hv[6], hv[7]))}, rws, hfo);
        if (s == 19) { hfo += 56 * 1024; OPQ(hfo); }
      }
      auto ldw2 = [&](int i) { w2r[i] = __builtin_amdgcn_raw_buffer_load_b64(rw2, w2o + (i & 3) * 896, (i >> 3) * 32 * 896 + ((i >> 2) & 1) * 3584, 0); };
      if (nt == 1 || nt == 2) {
        const int e = (nt - 1) * 28 + s;
#pragma unroll
        for (int i = 0; i < NW2E * 8; i++)
          if (i >= e * NW2E * 8 / 56 && i < (e + 1) * NW2E * 8 / 56) ldw2(i);
      }
      if (nt == 3) {
        constexpr int NR = 56 - NW2E * 8;
        if (s < NR / 2) { ldw2(NW2E * 8 + 2 * s); ldw2(NW2E * 8 + 2 * s + 1); }
        if (s >= 21) hf[0][s - 21] = ld128(rws, h2o, (s - 21) * 1024);
      }
      SCHED();
    }
    STAMP(13 + nt);
    if (nt == 0) {
      BAR();
      if (w < 4) stats(std::integral_constant<int, 7>{}, 2 + (w >> 1), 7 * (w & 1), w & 1);
      else stats(std::integral_constant<int, 14>{}, w, 0, 0);
    }
    if (nt == 1) BAR();
    if (nt == 2) waitflag(fb, rws, fbo, t0, t1);
    SCHED();
  }
  {
    getst(3);
    float hv[8];
#pragma unroll
    for (int h = 0; h < 2; h++) {
      const v4f c1 = *LDSP(v4f, csa + 16 * h), c3 = *LDSP(v4f, csa + 512 + 16 * h);
#pragma unroll
      for (int r = 0; r < 4; r++) hv[4 * h + r] = epi(ac[3][4 * h + r], ac[3][4 * h + r + 8], c1[r], c3[r]);
    }
    st128(v4u{canon(pk(hv[0], hv[1])), canon(pk(hv[2], hv[3])), canon(pk(hv[4], hv[5])), canon(pk(hv[6], hv[7]))}, rws, hfo);
  }
  FENCE();
#pragma unroll
  for (int g = 1; g < 3; g++)
#pragma unroll
    for (int s = 0; s < 7; s++) hf[g][s] = ld128(rws, h2o, (g * 56 + s) * 1024);
  FENCE();
  STAMP(17);
  PHASE();
  res = trd2<256>(((w >> 1) == 0 ? L_Q0 : L_Q1 + ((w >> 1) - 1) * 28672) + (64 * dt + 16 * (lane >> 4) + 8 * ((lane >> 1) & 1) + ((lane & 15) >> 2)) * 64 + 32 * q2 + 16 * (w & 1) + 8 * (lane & 1));
  v4u wa[7][4];
#pragma unroll
  for (int s = 0; s < 7; s++) {
    const v2u* r = w2r + 8 * s;
#pragma unroll
    for (int j = 0; j < 4; j++) {
      wa[s][0][j] = plo(r[2 * j].x, r[2 * j + 1].x); wa[s][1][j] = phi(r[2 * j].x, r[2 * j + 1].x);
      wa[s][2][j] = plo(r[2 * j].y, r[2 * j + 1].y); wa[s][3][j] = phi(r[2 * j].y, r[2 * j + 1].y);
    }
    SCHED();
  }
  const u32 pw = (u32)(w * 4096 + (lane ^ ((lane >> 1) & 8)) * 16);
  auto finalize = [&]() {
    const int n = 8 * (w & 1) + (lane & 7), kq = lane >> 4;
    const u32 a = (u32)((w >> 1) * 32768 + ((lane >> 3) & 1) * 2048 + (kq * 16 + (n ^ (8 * (kq & 1)))) * 16);
    v4f e = *LDSP(v4f, a), o = *LDSP(v4f, a + 1024);
#pragma unroll
    for (int u = 1; u < 8; u++) { e += *LDSP(v4f, a + u * 4096); o += *LDSP(v4f, a + u * 4096 + 1024); }
    const v4u ov = {pk(lo2f(res.x) + e.x, hi2f(res.x) + e.y), pk(lo2f(res.y) + e.z, hi2f(res.y) + e.w),
                    pk(lo2f(res.z) + o.x, hi2f(res.z) + o.y), pk(lo2f(res.w) + o.z, hi2f(res.w) + o.w)};
    *(v4u*)(gout + (128 * rt + 32 * (w >> 1) + 16 * q2 + n) * D + 64 * dt + 8 * (lane >> 3)) = ov;
  };
#pragma unroll
  for (int g = 0; g < 4; g++) {
    if (g == 3) {
      BAR();
      STAMP(22);
      if (w < 6) finalize();
      FENCE();
    }
    settle<7>(hf[g], rws, h2o + g * 56 * 1024, lin);
    STAMP(18 + g);
    v4f c[4];
#pragma unroll
    for (int i = 0; i < 4; i++) c[i] = v4f{0.f, 0.f, 0.f, 0.f};
#pragma unroll
    for (int s = 0; s < 7; s++)
#pragma unroll
      for (int i = 0; i < 4; i++) c[i] = MFMA16(wa[s][i], hf[g][s], c[i]);
    SCHED();
    if (g == 0) BAR();
    if (g == 1) {
#pragma unroll
      for (int s = 0; s < 7; s++) hf[3][s] = ld128(rws, h2o, (3 * 56 + s) * 1024);
      FENCE();
    }
#pragma unroll
    for (int r = 0; r < 4; r++) *LDSP(v4f, g * 32768 + pw + r * 1024) = v4f{c[0][r], c[1][r], c[2][r], c[3][r]};
    SCHED();
  }
  BAR();
  if (w >= 6) finalize();
  if (b == 0 && tid == 0) st64(t0, t1 ^ 0x5bd1e995u, rws, WS_EPOCH);
  STAMP(23);
  STOUT();
}

extern "C" void kernel_launch(void* const* d_in, const int* in_sizes, int n_in, void* d_out, int out_size, void* d_ws, size_t ws_size,
                              hipStream_t stream) {
  (void)hipFuncSetAttribute((const void*)fused_kernel, hipFuncAttributeMaxDynamicSharedMemorySize, L_TOTAL);
  fused_kernel<<<256, 512, L_TOTAL, stream>>>((const u16*)d_in[0], (const u16*)d_in[1], (const u16*)d_in[2], (const u16*)d_in[3],
                                              (const u16*)d_in[4], (const u16*)d_in[5], (const u16*)d_in[7], (const u16*)d_in[8],
                                              (const u16*)d_in[9], (u16*)d_out, (unsigned char*)d_ws);
}
```

```cpp
#include <hip/hip_runtime.h>
#include <hip/hip_bf16.h>
#include <type_traits>

typedef unsigned u32;
typedef unsigned short u16;
typedef unsigned long long u64;
typedef u32 v4u __attribute__((ext_vector_type(4)));
typedef u32 v2u __attribute__((ext_vector_type(2)));
typedef float v4f __attribute__((ext_vector_type(4)));
typedef float v2f __attribute__((ext_vector_type(2)));
typedef float v16f __attribute__((ext_vector_type(16)));
typedef __bf16 bf8 __attribute__((ext_vector_type(8)));
typedef __bf16 bf2 __attribute__((ext_vector_type(2)));
typedef short s4 __attribute__((ext_vector_type(4)));
typedef __amdgpu_buffer_rsrc_t rsrc_t;
#define LDSP(T, off) ((__attribute__((address_space(3))) T*)(size_t)(off))

constexpr int S = 2048, D = 448, F = 1792;
constexpr u32 WS_EPOCH = 0;
constexpr u32 WS_FLAG = 4096;
constexpr u32 WS_CS = 16384;
constexpr u32 WS_KVG = 49152;
constexpr u32 WS_XQ = WS_KVG + 256u * 224 * 64;
constexpr u32 WS_HT = WS_XQ + 256u * 224 * 64;
constexpr u32 WS_HF = WS_HT + 32u * 448 * 128;
constexpr u32 WS_END = WS_HF + 128u * 56 * 1024;
constexpr u32 WS_DBG = WS_END;
constexpr u32 L_W = 0;
constexpr u32 L_TAP = 114688;
constexpr u32 L_RX = 147968;
constexpr u32 L_DUM = 157184;
constexpr u32 L_STAT = 162816, L_CNT = L_STAT + 64;
constexpr u32 L_RED = 114688;
constexpr u32 L_Q0 = 114688, L_Q1 = 0;
constexpr u32 L_ST = 143360;
constexpr u32 L_CSV = 145408;
constexpr u32 L_TOTAL = 160 * 1024;
constexpr u32 OOB = 0x80000000u;
constexpr int SC1 = 16;
constexpr u32 SPIN_MAX = 1u << 16;

#define FENCE() asm volatile("" ::: "memory")
#define WAITV(n) asm volatile("s_waitcnt vmcnt(" #n ")" ::: "memory")
#define DRAINV() __builtin_amdgcn_s_waitcnt(0x0F70)
#define BAR() do { asm volatile("s_waitcnt lgkmcnt(0)" ::: "memory"); __builtin_amdgcn_s_barrier(); FENCE(); } while (0)
#define SCHED() __builtin_amdgcn_sched_barrier(0)
#define PHASE() do { SCHED(); asm volatile("" : "+v"(tid)); lane = tid & 63; SCHED(); } while (0)
#define UNLIKELY(x) __builtin_expect(!!(x), 0)
#define USE(x) asm volatile("" ::"v"(x))
#define OPQ(x) asm volatile("" : "+v"(x))

__device__ __forceinline__ rsrc_t mk(const void* p, u32 bytes) { return __builtin_amdgcn_make_buffer_rsrc((void*)p, 0, bytes, 0x00020000); }
__device__ __forceinline__ v4u mkv(const void* p, u32 bytes) { const u64 a = (u64)p; return v4u{(u32)a, (u32)(a >> 32) & 0xffffu, bytes, 0x00020000u}; }
__device__ __forceinline__ float lo2f(u32 v) { return __builtin_bit_cast(float, v << 16); }
__device__ __forceinline__ float hi2f(u32 v) { return __builtin_bit_cast(float, v & 0xffff0000u); }
__device__ __forceinline__ u32 pk(float a, float b) { return __builtin_bit_cast(u32, __builtin_convertvector(v2f{a, b}, bf2)); }
__device__ __forceinline__ u32 plo(u32 a, u32 b) { return __builtin_amdgcn_perm(b, a, 0x05040100u); }
__device__ __forceinline__ u32 phi(u32 a, u32 b) { return __builtin_amdgcn_perm(b, a, 0x07060302u); }
__device__ __forceinline__ u32 canon(u32 v) { return min(v, 0xFFFFFFFEu); }
__device__ __forceinline__ u32 max4(v4u t) { return max(max(t.x, t.y), max(t.z, t.w)); }
__device__ __forceinline__ u32 max5(u32 m, v4u t) { return max(max(max(m, t.x), t.y), max(t.z, t.w)); }
__device__ __forceinline__ bf8 asbf(v4u v) { return __builtin_bit_cast(bf8, v); }
__device__ __forceinline__ v2u trd(u32 a) { return __builtin_bit_cast(v2u, __builtin_amdgcn_ds_read_tr16_b64_v4i16(LDSP(s4, a))); }
template <int O> __device__ __forceinline__ v4u trd2(u32 a) { const v2u x = trd(a), y = trd(a + O); return v4u{x.x, x.y, y.x, y.y}; }
__device__ __forceinline__ void st128(v4u v, rsrc_t r, u32 off) { __builtin_amdgcn_raw_buffer_store_b128(v, r, off, 0, SC1); }
__device__ __forceinline__ void st64(u32 a, u32 b_, rsrc_t r, u32 off) { __builtin_amdgcn_raw_buffer_store_b64(v2u{a, b_}, r, off, 0, SC1); }
__device__ __forceinline__ v4u ld128(rsrc_t r, u32 off, u32 so) { return __builtin_amdgcn_raw_buffer_load_b128(r, off, so, SC1); }
__device__ __forceinline__ v2u ld64(rsrc_t r, u32 off, u32 so) { return __builtin_amdgcn_raw_buffer_load_b64(r, off, so, SC1); }
__device__ __forceinline__ u32 f2u(float v) { return __builtin_bit_cast(u32, v); }
__device__ __forceinline__ float u2f(u32 v) { return __builtin_bit_cast(float, v); }
__device__ __forceinline__ float opq(float v) { asm("" : "+v"(v)); return v; }
template <int CTRL> __device__ __forceinline__ float dppadd(float v) { return v + u2f(__builtin_amdgcn_update_dpp(0, f2u(v), CTRL, 0xF, 0xF, true)); }
__device__ __forceinline__ float swap16add(float a, float b) { const auto r = __builtin_amdgcn_permlane16_swap(f2u(a), f2u(b), false, false); return u2f(r[0]) + u2f(r[1]); }
__device__ __forceinline__ float swap32add(float a, float b) { const auto r = __builtin_amdgcn_permlane32_swap(f2u(a), f2u(b), false, false); return u2f(r[0]) + u2f(r[1]); }
__device__ __forceinline__ float wsum(float v) {
  v = dppadd<0xB1>(v); v = dppadd<0x4E>(v); v = dppadd<0x141>(v); v = dppadd<0x140>(v);
  v = swap16add(v, v);
  return swap32add(v, v);
}
template <u32 LI, u32 SO> __device__ __forceinline__ void dma16(u32 lb, u32 vo, v4u r) {
  u32 t;
  asm volatile("s_add_u32 m0, %1, %4\n\ts_mov_b32 %0, %5\n\tbuffer_load_dwordx4 %2, %3, %0 offen lds"
               : "=&s"(t) : "s"(lb), "v"(vo), "s"(r), "n"(LI), "n"(SO) : "memory", "scc");
}
__device__ __forceinline__ void warm(u32 vo, v4u r) {
  asm volatile("s_mov_b32 m0, %2\n\ts_nop 0\n\tbuffer_load_dword %0, %1, 0 offen lds" ::"v"(vo), "s"(r), "n"(L_DUM) : "memory");
}
template <int S0, int S1, int MS> __device__ __forceinline__ void dmaw(u32 lb, u32 vo, v4u r) {
  if constexpr (S0 < S1) { dma16<(S0 % 14) * 8192u + MS * 4096u, S0 * 57344u>(lb, vo, r); dmaw<S0 + 1, S1, MS>(lb, vo, r); }
}
template <int S0, int S1> __device__ __forceinline__ void dmaw2(u32 lb, u32 vo1, u32 vo3, v4u r1, v4u r3) {
  if constexpr (S0 < S1) { dma16<S0 * 8192u, S0 * 57344u>(lb, vo1, r1); dma16<S0 * 8192u + 4096u, S0 * 57344u>(lb, vo3, r3); dmaw2<S0 + 1, S1>(lb, vo1, vo3, r1, r3); }
}
template <int T> __device__ __forceinline__ void dmak(u32 lb, u32 vo, v4u r) {
  const u32 v1 = vo ^ 64u;
  dma16<0u, T * 28672u>(lb, vo, r); dma16<1024u, T * 28672u + 7168u>(lb, v1, r);
  dma16<2048u, T * 28672u + 14336u>(lb, vo, r); dma16<3072u, T * 28672u + 21504u>(lb, v1, r);
}
#define MFMA32(a, b, c) __builtin_amdgcn_mfma_f32_32x32x16_bf16(asbf(a), asbf(b), c, 0, 0, 0)
#define MFMA16(a, b, c) __builtin_amdgcn_mfma_f32_16x16x32_bf16(asbf(a), asbf(b), c, 0, 0, 0)
#define STAMP(i)
#define STOUT()
#define DLY1 0
#define DLY2 0
#define PSLP 4
#define HSLP 0
#define NE 2

template <int N, class SOF> __device__ __forceinline__ void settle(v4u* t, rsrc_t r, u32 off, SOF so) {
  u32 m = 0;
#pragma unroll
  for (int i = 0; i < N; i++) m = max5(m, t[i]);
  if (UNLIKELY(__any(m == 0xFFFFFFFFu))) {
#pragma nounroll
    for (u32 spin = 0; spin < SPIN_MAX; spin++) {
      bool any = false;
      FENCE();
#pragma unroll
      for (int i = 0; i < N; i++)
        if (__any(max4(t[i]) == 0xFFFFFFFFu)) { t[i] = ld128(r, off, so(i)); any = true; }
      if (!any) break;
    }
    DRAINV();
  }
}
__device__ __forceinline__ void waitflag(v2u f, rsrc_t r, u32 off, u32 t0, u32 t1) {
  if (UNLIKELY(__any((f.x != t0) | (f.y != t1)))) {
#pragma nounroll
    for (u32 spin = 0; spin < SPIN_MAX; spin++) {
      FENCE();
      f = ld64(r, off, 0);
      if (!__any((f.x != t0) | (f.y != t1))) break;
    }
    DRAINV();
  }
  FENCE();
}

__global__ void __launch_bounds__(512, 2)
fused_kernel(const u16* __restrict__ gx, const u16* __restrict__ gk, const u16* __restrict__ gv, const u16* __restrict__ gsk,
             const u16* __restrict__ gssm, const u16* __restrict__ gsd, const u16* __restrict__ gw1, const u16* __restrict__ gw3,
             const u16* __restrict__ gw2, u16* __restrict__ gout, unsigned char* __restrict__ ws) {
  asm volatile("" ::"s"(gx), "s"(gk), "s"(gv), "s"(gsk), "s"(gssm), "s"(gsd), "s"(gw1), "s"(gw3), "s"(gw2), "s"(gout), "s"(ws));
  int tid = threadIdx.x, lane = tid & 63;
  const int w = __builtin_amdgcn_readfirstlane(tid >> 6), b = blockIdx.x;
  const int xc = b & 7, ii = b >> 3;
  const bool mainwg = ii < 28;
  const int p = xc * 28 + ii;
  const int dt = ii % 7, q2 = xc & 1;
  const int rt = 4 * (xc >> 1) + ii / 7, fj = 7 * q2 + dt;
  const rsrc_t rws = mk(ws, WS_END);
  const v4u poison = {0xFFFFFFFFu, 0xFFFFFFFFu, 0xFFFFFFFFu, 0xFFFFFFFFu};
  STAMP(0);

  u32 t0, t1;
  {
    const int pp = tid & 255;
    const u32 colo = pp < 224 ? (u32)(8 * b * 896 + pp * 4) : OOB;
    auto kvpart = [&](auto mw) {
      constexpr bool MW = decltype(mw)::value;
      u32 ra[11], rb[8];
      const rsrc_t rk = mk(gk, S * D * 2), rv = mk(gv, S * D * 2);
      const u32 halo = (b > 0 && pp < 224) ? colo - 3 * 896 : OOB;
#pragma unroll
      for (int i = 0; i < 3; i++) ra[i] = __builtin_amdgcn_raw_buffer_load_b32(rk, halo + i * 896, 0, 0);
#pragma unroll
      for (int i = 0; i < 8; i++) ra[3 + i] = __builtin_amdgcn_raw_buffer_load_b32(rk, colo + (i & 3) * 896, (i >> 2) * 3584, 0);
      const v4u skw = __builtin_amdgcn_raw_buffer_load_b128(mk(gsk, D * 8), pp * 16, 0, 0);
#pragma unroll
      for (int i = 0; i < 8; i++) rb[i] = __builtin_amdgcn_raw_buffer_load_b32(rv, colo + (i & 3) * 896, (i >> 2) * 3584, 0);
      const v2u ep = ld64(rws, WS_EPOCH, 0);
      FENCE();
      __builtin_amdgcn_s_barrier();
      FENCE();
      USE(skw);
      STAMP(1);
      v4u tp[4], cs[28];
      if constexpr (MW) {
        const rsrc_t rt_ = mk(gssm, D * S * 2);
        const u32 o = (u32)(2 * p * 4096 + pp * 16);
#pragma unroll
        for (int c = 0; c < 2; c++) {
          tp[2 * c] = __builtin_amdgcn_raw_buffer_load_b128(rt_, pp > 0 ? o - 16 : OOB, c * 4096, 0);
          tp[2 * c + 1] = __builtin_amdgcn_raw_buffer_load_b128(rt_, o, c * 4096, 0);
        }
      }
      FENCE();
      const float s0[4] = {lo2f(skw.x), hi2f(skw.x), lo2f(skw.y), hi2f(skw.y)};
      const float s1[4] = {lo2f(skw.z), hi2f(skw.z), lo2f(skw.w), hi2f(skw.w)};
      float k0[8], k1[8];
#pragma unroll
      for (int i = 0; i < 8; i++) {
        float a0 = lo2f(ra[i + 3]) * s0[0], a1 = hi2f(ra[i + 3]) * s1[0];
#pragma unroll
        for (int j = 1; j < 4; j++) { a0 += lo2f(ra[i + 3 - j]) * s0[j]; a1 += hi2f(ra[i + 3 - j]) * s1[j]; }
        k0[i] = a0 * lo2f(rb[i]); k1[i] = a1 * hi2f(rb[i]);
      }
      *LDSP(v4u, L_RX + pp * 32) = v4u{pk(k0[3], k0[2]), pk(k0[1], k0[0]), pk(k0[7], k0[6]), pk(k0[5], k0[4])};
      *LDSP(v4u, L_RX + pp * 32 + 16) = v4u{pk(k1[3], k1[2]), pk(k1[1], k1[0]), pk(k1[7], k1[6]), pk(k1[5], k1[4])};
      t0 = __builtin_amdgcn_readfirstlane(ep.x) + 1u; t1 = __builtin_amdgcn_readfirstlane(ep.y) ^ 0x5bd1e995u;
      FENCE();
      v2u pl[4];
#pragma unroll
      for (int i = 0; i < 4; i++) pl[i] = *LDSP(v2u, L_RX + w * 2048 + lane * 8 + i * 512);
      const u32 o = WS_KVG + b * 14336 + w * 4096 + lane * 16;
#pragma unroll
      for (int i = 0; i < 4; i++)
        if (i < 2 || w < 3) st128(v4u{pl[i].x, pl[i].y, t0, t1}, rws, o + i * 1024);
      FENCE();
      STAMP(2);
      BAR();
      STAMP(3);
      if constexpr (MW) {
#pragma unroll
        for (int i = 0; i < 4; i++) OPQ(tp[i]);
#pragma unroll
        for (int c = 0; c < 2; c++) {
          const u32 a[8] = {tp[2 * c].x, tp[2 * c].y, tp[2 * c].z, tp[2 * c].w, tp[2 * c + 1].x, tp[2 * c + 1].y, tp[2 * c + 1].z, tp[2 * c + 1].w};
          u32 od[7];
#pragma unroll
          for (int j = 0; j < 7; j++) od[j] = __builtin_amdgcn_alignbit(a[j + 1], a[j], 16);
          const u32 base = L_TAP + c * 16640 + (pp + 1) * 16;
          *LDSP(v4u, base) = v4u{od[0], od[1], od[2], od[3]};
          *LDSP(v4u, base + 4160) = v4u{a[1], a[2], a[3], a[4]};
          *LDSP(v4u, base + 8320) = v4u{od[1], od[2], od[3], od[4]};
          *LDSP(v4u, base + 12480) = v4u{a[2], a[3], a[4], a[5]};
          if (pp == 255) {
            *LDSP(v2u, base + 16) = v2u{od[4], od[5]};
            *LDSP(v2u, base + 16 + 4160) = v2u{a[5], a[6]};
            *LDSP(v2u, base + 16 + 8320) = v2u{od[5], od[6]};
            *LDSP(v2u, base + 16 + 12480) = v2u{a[6], a[7]};
          }
        }
        if (tid < 8) *LDSP(v4u, L_TAP + tid * 4160) = v4u{0u, 0u, 0u, 0u};
      } else {
        BAR();
        const int j = b - 224;
        const rsrc_t rw = mk(j < 16 ? gw1 : gw3, D * F * 2);
        const u32 o = pp < 224 ? (u32)((pp / 14) * 28 * 3584 + (j & 15) * 224 + (pp % 14) * 16) : OOB;
#pragma unroll
        for (int i = 0; i < 28; i++) cs[i] = __builtin_amdgcn_raw_buffer_load_b128(rw, o, i * 3584, 0);
        float sm[8];
#pragma unroll
        for (int e = 0; e < 8; e++) sm[e] = 0.f;
#pragma unroll
        for (int i = 0; i < 28; i++)
#pragma unroll
          for (int e = 0; e < 4; e++) { sm[2 * e] += lo2f(cs[i][e]); sm[2 * e + 1] += hi2f(cs[i][e]); }
        *LDSP(v4f, pp * 32) = v4f{sm[0], sm[1], sm[2], sm[3]};
        *LDSP(v4f, pp * 32 + 16) = v4f{sm[4], sm[5], sm[6], sm[7]};
      }
    };
    if (w < 4) {
      if (mainwg) kvpart(std::true_type{}); else kvpart(std::false_type{});
    } else {
      {
        const u32 o = mainwg ? WS_HT + (u32)((8 * (w - 4) + (lane >> 4)) * 57344 + p * 256 + (lane & 15) * 16) : OOB;
        st128(poison, rws, o);
        st128(poison, rws, o + 4 * 57344);
      }
      FENCE();
      const rsrc_t rx = mk(gx, S * D * 2);
      const u32 so = lane < 56 ? (u32)((8 * b + 2 * (w - 4)) * 896 + lane * 16) : OOB;
      v4u sr[2];
      sr[0] = __builtin_amdgcn_raw_buffer_load_b128(rx, so, 0, 0);
      sr[1] = __builtin_amdgcn_raw_buffer_load_b128(rx, so + 896, 0, 0);
      u32 rb[8];
#pragma unroll
      for (int i = 0; i < 8; i++) rb[i] = __builtin_amdgcn_raw_buffer_load_b32(rx, colo + (i & 3) * 896, (i >> 2) * 3584, 0);
      const v2u ep = ld64(rws, WS_EPOCH, 0);
      FENCE();
      WAITV(11);
      __builtin_amdgcn_s_barrier();
      FENCE();
      const v4u r1 = mkv(gw1, D * F * 2), r3 = mkv(gw3, D * F * 2);
      const u32 wb = L_W + (w - 4) * 1024;
      const u32 wrow = (u32)((4 * (w - 4) + (lane >> 4)) * 3584 + fj * 256);
      const u32 vo1 = wrow + (((lane & 15) ^ (2 * (lane >> 4))) * 16), vo3 = vo1 ^ 128;
      USE(ep); USE(rb[7]);
      STAMP(1);
      if (mainwg) dmaw2<0, NE>(wb, vo1, vo3, r1, r3);
      float mean[2], rstd[2];
#pragma unroll
      for (int r = 0; r < 2; r++) {
        const float e[8] = {lo2f(sr[r].x), hi2f(sr[r].x), lo2f(sr[r].y), hi2f(sr[r].y), lo2f(sr[r].z), hi2f(sr[r].z), lo2f(sr[r].w), hi2f(sr[r].w)};
        const float s = ((e[0] + e[1]) + (e[2] + e[3])) + ((e[4] + e[5]) + (e[6] + e[7]));
        mean[r] = wsum(s) * (1.f / D);
        float q = 0.f;
#pragma unroll
        for (int i = 0; i < 8; i++) { const float d = e[i] - mean[r]; q += d * d; }
        q = wsum(lane < 56 ? q : 0.f);
        rstd[r] = __builtin_amdgcn_rsqf(q * (1.f / D) + 1e-5f);
      }
      if (lane == 0) *LDSP(v4f, L_STAT + (w - 4) * 16) = v4f{mean[0], rstd[0], mean[1], rstd[1]};
      if (tid == 256) *LDSP(u32, L_CNT) = 0u;
      t0 = __builtin_amdgcn_readfirstlane(ep.x) + 1u; t1 = __builtin_amdgcn_readfirstlane(ep.y) ^ 0x5bd1e995u;
      STAMP(2);
      BAR();
      STAMP(3);
      v4f sa[4];
#pragma unroll
      for (int i = 0; i < 4; i++) sa[i] = *LDSP(v4f, L_STAT + i * 16);
      const u32 xb = L_W + 12 * 8192 + (w - 4) * 1024;
#pragma unroll
      for (int h = 0; h < 2; h++) {
        const v4f a = sa[2 * h], c = sa[2 * h + 1];
        const u32 *x = rb + 4 * h;
        *LDSP(v4u, xb + (lane >> 4) * 4096 + (lane & 15) * 64 + h * 16) =
            v4u{plo(x[0], x[1]), plo(x[2], x[3]), pk((lo2f(x[0]) - a.x) * a.y, (lo2f(x[1]) - a.z) * a.w), pk((lo2f(x[2]) - c.x) * c.y, (lo2f(x[3]) - c.z) * c.w)};
        *LDSP(v4u, xb + (lane >> 4) * 4096 + (lane & 15) * 64 + 32 + h * 16) =
            v4u{phi(x[0], x[1]), phi(x[2], x[3]), pk((hi2f(x[0]) - a.x) * a.y, (hi2f(x[1]) - a.z) * a.w), pk((hi2f(x[2]) - c.x) * c.y, (hi2f(x[3]) - c.z) * c.w)};
      }
      FENCE();
      v4u px[4];
#pragma unroll
      for (int i = 0; i < 4; i++) px[i] = *LDSP(v4u, xb + i * 4096 + lane * 16);
      const u32 o = WS_XQ + b * 14336 + (w - 4) * 4096 + lane * 16;
#pragma unroll
      for (int i = 0; i < 4; i++)
        if (i < 2 || w < 7) st128(px[i], rws, o + i * 1024);
      FENCE();
      {
        const u32 ho = mainwg ? WS_HF + (u32)((rt * 8 * 56 + 4 * fj + (w - 4)) * 1024 + lane * 16) : OOB;
#pragma unroll
        for (int i = 0; i < 8; i++) st128(poison, rws, ho + i * 56 * 1024);
      }
      WAITV(8);
      STAMP(4);
      if (lane == 0) {
        const u32 old = __hip_atomic_fetch_add(LDSP(u32, L_CNT), 1u, __ATOMIC_RELAXED, __HIP_MEMORY_SCOPE_WORKGROUP);
        if (old == 3u) st64(t0, t1, rws, WS_FLAG + b * 8);
      }
      FENCE();
      if (mainwg) dmaw2<NE, 14>(wb, vo1, vo3, r1, r3);
      if (!mainwg) BAR();
      STAMP(5);
    }
  }
  PHASE();
  if (!mainwg) {
    BAR();
    if (w > 0) {
      if (HSLP) __builtin_amdgcn_s_sleep(HSLP);
      const v4u rw2 = mkv(gw2, F * D * 2);
      const int q = tid - 64, r = q / 7;
      const u32 o = (u32)(((r >> 3) * 224 + (ii - 28) * 8 + (r & 7)) * 896 + (q % 7) * 128);
#pragma unroll
      for (int t = 0; t < 7; t++) warm(o + t * 28672, rw2);
      WAITV(0);
    }
    if (w == 0) {
      v2f s = {0.f, 0.f};
#pragma unroll
      for (int g = 0; g < 16; g++) s += *LDSP(v2f, g * 448 + lane * 8);
      st128(v4u{f2u(s.x), f2u(s.y), t0, t1}, rws, lane < 56 ? WS_CS + (b - 224) * 896 + lane * 16 : OOB);
    }
    STAMP(23);
    STOUT();
    return;
  }

  if (w < 4) {
    STAMP(4);
    if (DLY1) __builtin_amdgcn_s_sleep(DLY1);
    v4u g[4];
    const u32 o = WS_KVG + (tid >> 2) * 14336 + p * 64 + (tid & 3) * 16;
#pragma unroll
    for (int i = 0; i < 4; i++) g[i] = ld128(rws, o, i * 64 * 14336);
#pragma nounroll
    for (u32 spin = 0; spin < SPIN_MAX; spin++) {
      bool bad[4], any = false;
#pragma unroll
      for (int i = 0; i < 4; i++) { bad[i] = (g[i].z != t0) | (g[i].w != t1); any |= bad[i]; }
      if (!__any(any)) break;
      __builtin_amdgcn_s_sleep(PSLP);
      FENCE();
#pragma unroll
      for (int i = 0; i < 4; i++) if (bad[i]) g[i] = ld128(rws, o, i * 64 * 14336);
    }
    STAMP(5);
    const u32 la = L_RX + ((tid >> 1) & 1) * 4608 + (508 - 8 * (tid >> 2) - 4 * (tid & 1)) * 2;
#pragma unroll
    for (int i = 0; i < 4; i++) *LDSP(v2u, la + (3 - i) * 1024) = v2u{g[i].x, g[i].y};
    if (tid < 64) *LDSP(v4u, L_RX + (tid >> 5) * 4608 + 4096 + (tid & 31) * 16) = v4u{0u, 0u, 0u, 0u};
  }
  const int ch = w >> 2, wq = w & 3;
  const float dval = lo2f(gsd[2 * p + ch]);
  const u32 fo1 = WS_FLAG + (64 * wq + lane) * 8;
  BAR();
  STAMP(6);
  v4u w13[28];
  const u32 wra = L_W + (((lane >> 4) & 1) * 4 + 2 * (lane >> 5)) * 1024 + ((lane & 15) >> 2) * 256 +
                  (((2 * w + (lane & 1)) ^ (2 * ((lane & 15) >> 2)) ^ (8 * ((lane >> 4) & 1))) * 16) + ((lane >> 1) & 1) * 8;
  {
    const v2u f1 = ld64(rws, fo1, 0);
    FENCE();
    const int la_ = lane & 15, kg = lane >> 4;
    v4f acc[8];
#pragma unroll
    for (int n = 0; n < 8; n++) acc[n] = v4f{0.f, 0.f, 0.f, 0.f};
    u32 aa = L_TAP + ch * 16640 + (la_ & 3) * 4160 + ((la_ >> 2) + 2 * kg + 16 * wq) * 8, ab = aa + 8;
    asm volatile("" : "+v"(ab));
    const u32 ba = L_RX + ch * 4608 + (30 + 8 * wq - 2 * la_ + kg) * 16;
    auto lda = [&](int o) { const v2u x = *LDSP(v2u, aa + o), y = *LDSP(v2u, ab + o); return v4u{x.x, x.y, y.x, y.y}; };
    v4u A[8], B[8], A2[8], B2[8];
#pragma unroll
    for (int m = 0; m < 8; m++) { A[m] = lda(m * 512); B[m] = *LDSP(v4u, ba + (7 - m) * 512); SCHED(); }
#pragma unroll
    for (int g = 0; g < 8; g++) {
      A2[g] = lda(64 + g * 512); B2[g] = *LDSP(v4u, ba + 64 + (7 - g) * 512);
#pragma unroll
      for (int m = 0; m <= g; m++) {
        if (m + g < 8) acc[m + g] = MFMA16(A[m], B[g], acc[m + g]);
        if (m < g && m + g < 8) acc[m + g] = MFMA16(A[g], B[m], acc[m + g]);
      }
      SCHED();
    }
    const int t = 512 * wq + 16 * la_ + 4 * kg;
    const v2u kv0 = *LDSP(v2u, L_RX + ch * 4608 + (2044 - t) * 2), kv1 = *LDSP(v2u, L_RX + ch * 4608 + (2044 - 256 - t) * 2);
    waitflag(f1, rws, fo1, t0, t1);
    WAITV(0);
    STAMP(7);
    BAR();
    STAMP(8);
    const u32 xqo = WS_XQ + (64 * wq + 2 * la_ + (kg >> 1)) * 14336 + p * 64 + ch * 32 + (kg & 1) * 16;
    const v4u xq0 = ld128(rws, xqo, 0), xq1 = ld128(rws, xqo, 32 * 14336);
    FENCE();
#pragma unroll
    for (int d = 7; d >= 0; d--) {
      if (d > 0) { w13[14 - 2 * d] = trd2<1024>(wra + (14 - 2 * d) * 8192); w13[15 - 2 * d] = trd2<1024>(wra + (15 - 2 * d) * 8192); }
#pragma unroll
      for (int m = 0; m <= d; m++) acc[d] = MFMA16(A2[m], B2[d - m], acc[d]);
      SCHED();
    }
#pragma unroll
    for (int n = 7; n >= 0; n--)
      if ((n >> 1) != wq) *LDSP(v4f, L_RED + (w * 6 + (n < 2 * wq ? n : n - 2)) * 1024 + lane * 16) = acc[n];
    v4f y0, y1;
#pragma unroll
    for (int n = 0; n < 4; n++) if (n == wq) { y0 = acc[2 * n]; y1 = acc[2 * n + 1]; }
    BAR();
    STAMP(9);
#pragma unroll
    for (int sq = 1; sq < 4; sq++) {
      const int sw = (wq + sq) & 3;
      const u32 a = L_RED + ((ch * 4 + sw) * 6 + (wq < sw ? 2 * wq : 2 * wq - 2)) * 1024 + lane * 16;
      y0 += *LDSP(v4f, a);
      y1 += *LDSP(v4f, a + 1024);
    }
    const u32 h0x = pk(lo2f(xq0.x) + lo2f(xq0.z) * (y0.x + hi2f(kv0.y) * dval), hi2f(xq0.x) + hi2f(xq0.z) * (y0.y + lo2f(kv0.y) * dval));
    const u32 h0y = pk(lo2f(xq0.y) + lo2f(xq0.w) * (y0.z + hi2f(kv0.x) * dval), hi2f(xq0.y) + hi2f(xq0.w) * (y0.w + lo2f(kv0.x) * dval));
    const u32 h1x = pk(lo2f(xq1.x) + lo2f(xq1.z) * (y1.x + hi2f(kv1.y) * dval), hi2f(xq1.x) + hi2f(xq1.z) * (y1.y + lo2f(kv1.y) * dval));
    const u32 h1y = pk(lo2f(xq1.y) + lo2f(xq1.w) * (y1.z + hi2f(kv1.x) * dval), hi2f(xq1.y) + hi2f(xq1.w) * (y1.w + lo2f(kv1.x) * dval));
    const int s1 = (wq + 1) & 3;
    const u32 tb = L_RED + ((ch * 4 + s1) * 6 + (wq < s1 ? 2 * wq : 2 * wq - 2)) * 1024;
    *LDSP(v2u, tb + la_ * 32 + kg * 8) = v2u{canon(h0x), canon(h0y)};
    *LDSP(v2u, tb + 512 + la_ * 32 + kg * 8) = v2u{canon(h1x), canon(h1y)};
    FENCE();
    st128(*LDSP(v4u, tb + lane * 16), rws, WS_HT + (u32)((8 * wq + (lane >> 3)) * 57344 + (2 * p + ch) * 128 + (lane & 7) * 16));
  }
  FENCE();
  STAMP(10);
  PHASE();

  {
    const v4u rw = mkv(w < 4 ? gw1 : gw3, D * F * 2);
    const u32 vo = (u32)((4 * wq + (lane >> 4)) * 3584 + fj * 256) + (((lane & 15) ^ (2 * (lane >> 4)) ^ (8 * ch)) * 16);
    dmaw<14, 28, 0>(L_W + w * 1024, vo, rw);
  }
  int hh = lane >> 5;
  u32 hfo = WS_HF + (u32)(((rt * 2 + ((lane >> 4) & 1)) * 4 * 56 + 4 * fj + (w >> 1)) * 1024 + ((2 * (w & 1) + hh) * 16 + (lane & 15)) * 16);
  OPQ(hfo);
  FENCE();
  if (DLY2) __builtin_amdgcn_s_sleep(DLY2);
  v4u hc[14];
  const u32 hto = WS_HT + rt * 114688 - 8192 + (tid >> 2) * 128 + (tid & 3) * 16;
  const u32 so3 = w < 4 ? 57344u : 64u;
  auto hso = [&](int i) -> u32 { const int j = i % 7; return (i / 7) * 57344u + (j < 3 ? 8192u + j * 16384u : j == 3 ? so3 : 64u + 16384u * (j - 3)); };
#pragma unroll
  for (int i = 0; i < 4; i++) hc[i] = ld128(rws, hto, hso(i));
  FENCE();
  settle<4>(hc, rws, hto, hso);
  WAITV(0);
#pragma unroll
  for (int i = 4; i < 14; i++) hc[i] = ld128(rws, hto, hso(i));
  const u32 cso = w >= 6 ? WS_CS + (u32)((w - 6) * 14336 + fj * 1024 + lane * 16) : OOB;
  v4u csl = ld128(rws, cso, 0);
  FENCE();
  STAMP(11);
  BAR();
#pragma unroll
  for (int s = 14; s < 28; s++) w13[s] = trd2<1024>(wra + (s - 14) * 8192);
#pragma unroll
  for (int i = 0; i < 3; i++) *LDSP(v4u, L_Q0 + tid * 16 + i * 8192) = hc[i];
  if (w < 4) *LDSP(v4u, L_Q0 + tid * 16 + 24576) = hc[3];
  BAR();
  STAMP(12);
  const v4u ones = {0x3F803F80u, 0x3F803F80u, 0x3F803F80u, 0x3F803F80u};
  const u32 trb = (8 * hh + ((lane & 15) >> 2)) * 64 + ((lane >> 4) & 1) * 32 + (lane & 3) * 8;
  const u32 lw = L_Q1 + 4096 + tid * 16;
  auto stats = [&](auto ns, int rg, int s0, int slot) {
    constexpr int NS = decltype(ns)::value;
    const u32 sb = (rg < 2 ? L_Q0 : L_Q1 + ((rg >> 1) - 1) * 28672) + s0 * 2048 + (8 * (lane >> 4) + ((lane & 15) >> 2)) * 64 + (rg & 1) * 32 + (lane & 3) * 8;
    v4f g = {0.f, 0.f, 0.f, 0.f}, sm = {0.f, 0.f, 0.f, 0.f};
    v4u fr[NS];
#pragma unroll
    for (int s = 0; s < NS; s++) fr[s] = trd2<256>(sb + s * 2048);
#pragma unroll
    for (int s = 0; s < NS; s++) { g = MFMA16(fr[s], fr[s], g); sm = MFMA16(ones, fr[s], sm); }
    const int i = lane & 3;
    const float sq = i == 0 ? g.x : i == 1 ? g.y : i == 2 ? g.z : g.w;
    if (((lane & 15) >> 2) == (lane >> 4)) {
      const u32 a = L_ST + (16 * rg + (lane & 15)) * 16;
      if (NS == 14) *LDSP(v4f, a) = v4f{sm.x, sq, 0.f, 0.f}; else *LDSP(v2f, a + slot * 8) = v2f{sm.x, sq};
    }
  };
  float nm, ny, cv;
  auto getst = [&](int nt) {
    const v4f s = *LDSP(v4f, L_ST + (32 * nt + (lane & 31)) * 16);
    nm = opq(s.x + s.z) * (-1.f / D);
    cv = fmaxf(__builtin_fmaf(-nm, nm, opq(s.y + s.w) * (1.f / D)), 0.f) + 1e-5f;
    ny = -1.4426950408889634f * __builtin_amdgcn_rsqf(cv);
  };
  auto epi = [&](float a, float u, float c1, float c3) -> float {
    const float g = opq(__builtin_fmaf(nm, c1, a)), uu = opq(__builtin_fmaf(nm, c3, u));
    const float e = __builtin_amdgcn_exp2f(opq(g * ny));
    return opq(opq(g * uu) * __builtin_amdgcn_rcpf(opq(__builtin_fmaf(e, cv, cv))));
  };
  if (w < 4) stats(std::integral_constant<int, 7>{}, w >> 1, 7 * (w & 1), w & 1);
  SCHED();
  const v4u rw2 = mkv(gw2, F * D * 2);
  u32 w2o = (u32)((224 * w + (lane >> 3)) * 896 + dt * 128 + ((lane & 7) ^ ((lane >> 3) & 2)) * 16);
  u32 w2a = (u32)((lane >> 4) * 1024 + ((lane & 15) >> 2) * 128 + (((lane >> 3) & 1) | ((lane >> 3) & 2)) * 32 + (lane & 3) * 8);
  OPQ(w2o); OPQ(w2a);
  const u32 slA = w < 7 ? 86016u + w * 4096u : 147456u, slB = w < 7 ? L_Q0 + w * 4096u : 151552u, slC = w < 7 ? L_Q1 + w * 4096u : 155648u;
  v4u wa[7][4];
  auto rdw = [&](int t, u32 sl) {
    const u32 a = w2a + sl;
#pragma unroll
    for (int j = 0; j < 4; j++) wa[t][j] = trd2<512>(a ^ (32u * j));
  };
  auto usew = [&](int t) { asm volatile("" ::"v"(wa[t][0]), "v"(wa[t][1]), "v"(wa[t][2]), "v"(wa[t][3])); };
  auto rdres = [&]() {
    return trd2<256>(((w >> 1) == 0 ? L_Q0 : L_Q1 + ((w >> 1) - 1) * 28672) + (64 * dt + 16 * (lane >> 4) + 8 * ((lane >> 1) & 1) + ((lane & 15) >> 2)) * 64 + 32 * q2 + 16 * (w & 1) + 8 * (lane & 1));
  };
  const u32 h2o = WS_HF + (u32)(((rt * 2 + q2) * 4 * 56 + 7 * w) * 1024 + lane * 16);
  const u32 csa = L_CSV + (16 * w + 8 * hh) * 4;
  auto lin = [](int i) -> u32 { return i * 1024u; };
  v4u hf[4][7], res;
  v16f ac[4];
#define PDX 4
  constexpr int PD = PDX;
#pragma unroll
  for (int nt = 0; nt < 4; nt++) {
    u32 tb_ = trb + (nt == 0 ? L_Q0 : L_Q1 + (nt - 1) * 28672);
    OPQ(tb_);
    v4u bfr[28];
#pragma unroll
    for (int s = 0; s < PD; s++) bfr[s] = trd2<256>(tb_ + s * 1024);
    float hv[8];
    v4f c1, c3;
    if (nt > 0) getst(nt - 1);
#pragma unroll
    for (int r = 0; r < 16; r++) ac[nt][r] = 0.f;
    SCHED();
#pragma unroll
    for (int s = 0; s < 28; s++) {
      if (s + PD < 28) bfr[s + PD] = trd2<256>(tb_ + (s + PD) * 1024);
      if (nt > 0 && (s == 0 || s == 9)) { c1 = *LDSP(v4f, csa + (s ? 16 : 0)); c3 = *LDSP(v4f, csa + 512 + (s ? 16 : 0)); }
      ac[nt] = MFMA32(w13[s], bfr[s], ac[nt]);
      if (nt == 0) {
        if (s == 8) settle<3>(hc + 4, rws, hto, [&](int i) { return hso(i + 4); });
        if (s == 7 && w >= 4) *LDSP(v4u, L_Q1 + (tid & 255) * 16) = hc[3];
        if (s >= 8 && s < 11) *LDSP(v4u, lw + (s - 8) * 8192) = hc[s - 4];
        if (s == 15) settle<4>(hc + 7, rws, hto, [&](int i) { return hso(i + 7); });
        if (s >= 15 && s < 19) *LDSP(v4u, lw + (s - 12) * 8192) = hc[s - 8];
        if (s == 22) settle<3>(hc + 11, rws, hto, [&](int i) { return hso(i + 11); });
        if (s >= 22 && s < 25) *LDSP(v4u, lw + (s - 15) * 8192) = hc[s - 11];
      } else {
        if (s >= 2 && s < 18 && (s & 1) == 0) { const int r = (s - 2) >> 1; hv[r] = epi(ac[nt - 1][r], ac[nt - 1][r + 8], c1[r & 3], c3[r & 3]); }
        if (s == 18) st128(v4u{canon(pk(hv[0], hv[1])), canon(pk(hv[2], hv[3])), canon(pk(hv[4], hv[5])), canon(pk(hv[6], hv[7]))}, rws, hfo);
        if (s == 19) { hfo += 56 * 1024; OPQ(hfo); }
      }
      if (nt == 0 && s == 25) {
        if (w >= 6) {
          if (UNLIKELY(__any((csl.z != t0) | (csl.w != t1)))) {
#pragma nounroll
            for (u32 spin = 0; spin < SPIN_MAX; spin++) {
              FENCE();
              csl = ld128(rws, cso, 0);
              if (!__any((csl.z != t0) | (csl.w != t1))) break;
            }
            DRAINV();
          }
          *LDSP(v2u, L_CSV + (tid - 384) * 8) = v2u{csl.x, csl.y};
        }
        dmak<0>(slA, w2o, rw2);
      }
      if (nt == 1) {
        if (s == 0) dmak<1>(slB, w2o, rw2);
        if (s == 6) { WAITV(4); rdw(0, slA); }
        if (s == 9) { usew(0); dmak<2>(slA, w2o, rw2); }
        if (s == 16) { WAITV(4); rdw(1, slB); }
        if (s == 19) { usew(1); dmak<3>(slB, w2o, rw2); }
      }
      if (nt == 2) {
        if (s == 0) dmak<4>(slC, w2o, rw2);
        if (s == 4) { WAITV(9); rdw(2, slA); }
        if (s == 7) { usew(2); dmak<5>(slA, w2o, rw2); }
      }
      if (nt == 3) {
        if (s == 4) { WAITV(1); rdw(5, slA); }
        if (s == 7) { usew(5); dmak<6>(slA, w2o, rw2); }
        if (s == 8) rdw(3, slB);
        if (s == 12) rdw(4, slC);
        if (s >= 13 && s < 20) hf[0][s - 13] = ld128(rws, h2o, (s - 13) * 1024);
        if (s >= 20 && s < 27) hf[1][s - 20] = ld128(rws, h2o, (56 + s - 20) * 1024);
      }
      SCHED();
    }
    STAMP(13 + nt);
    if (nt == 0) {
      if (w < 2) res = rdres();
      BAR();
      if (w >= 2) res = rdres();
      if (w < 4) stats(std::integral_constant<int, 7>{}, 2 + (w >> 1), 7 * (w & 1), w & 1);
      else stats(std::integral_constant<int, 14>{}, w, 0, 0);
    }
    if (nt == 1) BAR();
    SCHED();
  }
  {
    getst(3);
    float hv[8];
#pragma unroll
    for (int h = 0; h < 2; h++) {
      const v4f c1 = *LDSP(v4f, csa + 16 * h), c3 = *LDSP(v4f, csa + 512 + 16 * h);
#pragma unroll
      for (int r = 0; r < 4; r++) hv[4 * h + r] = epi(ac[3][4 * h + r], ac[3][4 * h + r + 8], c1[r], c3[r]);
    }
    st128(v4u{canon(pk(hv[0], hv[1])), canon(pk(hv[2], hv[3])), canon(pk(hv[4], hv[5])), canon(pk(hv[6], hv[7]))}, rws, hfo);
  }
  FENCE();
#pragma unroll
  for (int s = 0; s < 7; s++) hf[2][s] = ld128(rws, h2o, (2 * 56 + s) * 1024);
  FENCE();
  WAITV(23);
  rdw(6, slA);
  STAMP(17);
  PHASE();
  const u32 pw = (u32)(w * 4096 + lane * 16);
  auto finalize = [&]() {
    const int n = 8 * (w & 1) + (lane & 7);
    const u32 a = (u32)((w >> 1) * 32768 + (lane >> 3) * 512 + (n ^ ((lane >> 1) & 8)) * 16);
    v4f e = *LDSP(v4f, a), o = *LDSP(v4f, a + 256);
#pragma unroll
    for (int u = 1; u < 8; u++) { e += *LDSP(v4f, a + u * 4096); o += *LDSP(v4f, a + u * 4096 + 256); }
    const v4u ov = {pk(lo2f(res.x) + e.x, hi2f(res.x) + e.y), pk(lo2f(res.y) + e.z, hi2f(res.y) + e.w),
                    pk(lo2f(res.z) + o.x, hi2f(res.z) + o.y), pk(lo2f(res.w) + o.z, hi2f(res.w) + o.w)};
    *(v4u*)(gout + (128 * rt + 32 * (w >> 1) + 16 * q2 + n) * D + 64 * dt + 8 * (lane >> 3)) = ov;
  };
#pragma unroll
  for (int g = 0; g < 4; g++) {
    if (g == 3) {
      BAR();
      STAMP(22);
      if (w < 6) finalize();
      FENCE();
    }
    settle<7>(hf[g], rws, h2o + g * 56 * 1024, lin);
    STAMP(18 + g);
    v4f c[4];
#pragma unroll
    for (int i = 0; i < 4; i++) c[i] = v4f{0.f, 0.f, 0.f, 0.f};
#pragma unroll
    for (int s = 0; s < 7; s++)
#pragma unroll
      for (int i = 0; i < 4; i++) c[i] = MFMA16(wa[s][i], hf[g][s], c[i]);
    SCHED();
    if (g == 0) BAR();
    if (g == 1) {
#pragma unroll
      for (int s = 0; s < 7; s++) hf[3][s] = ld128(rws, h2o, (3 * 56 + s) * 1024);
      FENCE();
    }
#pragma unroll
    for (int i = 0; i < 4; i++) *LDSP(v4f, g * 32768 + (pw ^ (128u * (i & 1))) + i * 1024) = c[i];
    SCHED();
  }
  BAR();
  if (w >= 6) finalize();
  if (b == 0 && tid == 0) st64(t0, t1 ^ 0x5bd1e995u, rws, WS_EPOCH);
  STAMP(23);
  STOUT();
}

extern "C" void kernel_launch(void* const* d_in, const int* in_sizes, int n_in, void* d_out, int out_size, void* d_ws, size_t ws_size,
                              hipStream_t stream) {
  (void)hipFuncSetAttribute((const void*)fused_kernel, hipFuncAttributeMaxDynamicSharedMemorySize, L_TOTAL);
  fused_kernel<<<256, 512, L_TOTAL, stream>>>((const u16*)d_in[0], (const u16*)d_in[1], (const u16*)d_in[2], (const u16*)d_in[3],
                                              (const u16*)d_in[4], (const u16*)d_in[5], (const u16*)d_in[7], (const u16*)d_in[8],
                                              (const u16*)d_in[9], (u16*)d_out, (unsigned char*)d_ws);
}
```

```cpp
#define TW_AT 23
#define TW_T 3500u
#include <hip/hip_runtime.h>
#include <hip/hip_bf16.h>
#include <type_traits>

typedef unsigned u32;
typedef unsigned short u16;
typedef unsigned long long u64;
typedef u32 v4u __attribute__((ext_vector_type(4)));
typedef u32 v2u __attribute__((ext_vector_type(2)));
typedef float v4f __attribute__((ext_vector_type(4)));
typedef float v2f __attribute__((ext_vector_type(2)));
typedef float v16f __attribute__((ext_vector_type(16)));
typedef __bf16 bf8 __attribute__((ext_vector_type(8)));
typedef __bf16 bf2 __attribute__((ext_vector_type(2)));
typedef short s4 __attribute__((ext_vector_type(4)));
typedef __amdgpu_buffer_rsrc_t rsrc_t;
#define LDSP(T, off) ((__attribute__((address_space(3))) T*)(size_t)(off))

constexpr int S = 2048, D = 448, F = 1792;
constexpr u32 WS_EPOCH = 0;
constexpr u32 WS_FLAG = 4096;
constexpr u32 WS_CS = 16384;
constexpr u32 WS_KVG = 49152;
constexpr u32 WS_XQ = WS_KVG + 256u * 224 * 64;
constexpr u32 WS_HT = WS_XQ + 256u * 224 * 64;
constexpr u32 WS_HF = WS_HT + 32u * 448 * 128;
constexpr u32 WS_END = WS_HF + 128u * 56 * 1024;
constexpr u32 WS_DBG = WS_END;
constexpr u32 L_W = 0;
constexpr u32 L_TAP = 114688;
constexpr u32 L_RX = 147968;
constexpr u32 L_DUM = 157184;
constexpr u32 L_STAT = 162816, L_CNT = L_STAT + 64;
constexpr u32 L_RED = 114688;
constexpr u32 L_Q0 = 114688, L_Q1 = 0;
constexpr u32 L_ST = 143360;
constexpr u32 L_CSV = 145408;
constexpr u32 L_TOTAL = 160 * 1024;
constexpr u32 OOB = 0x80000000u;
constexpr int SC1 = 16;
constexpr u32 SPIN_MAX = 1u << 16;

#define FENCE() asm volatile("" ::: "memory")
#define WAITV(n) asm volatile("s_waitcnt vmcnt(" #n ")" ::: "memory")
#define DRAINV() __builtin_amdgcn_s_waitcnt(0x0F70)
#define BAR() do { asm volatile("s_waitcnt lgkmcnt(0)" ::: "memory"); __builtin_amdgcn_s_barrier(); FENCE(); } while (0)
#define SCHED() __builtin_amdgcn_sched_barrier(0)
#define PHASE() do { SCHED(); asm volatile("" : "+v"(tid)); lane = tid & 63; SCHED(); } while (0)
#define UNLIKELY(x) __builtin_expect(!!(x), 0)
#define USE(x) asm volatile("" ::"v"(x))
#define OPQ(x) asm volatile("" : "+v"(x))

__device__ __forceinline__ rsrc_t mk(const void* p, u32 bytes) { return __builtin_amdgcn_make_buffer_rsrc((void*)p, 0, bytes, 0x00020000); }
__device__ __forceinline__ v4u mkv(const void* p, u32 bytes) { const u64 a = (u64)p; return v4u{(u32)a, (u32)(a >> 32) & 0xffffu, bytes, 0x00020000u}; }
__device__ __forceinline__ float lo2f(u32 v) { return __builtin_bit_cast(float, v << 16); }
__device__ __forceinline__ float hi2f(u32 v) { return __builtin_bit_cast(float, v & 0xffff0000u); }
__device__ __forceinline__ u32 pk(float a, float b) { return __builtin_bit_cast(u32, __builtin_convertvector(v2f{a, b}, bf2)); }
__device__ __forceinline__ u32 plo(u32 a, u32 b) { return __builtin_amdgcn_perm(b, a, 0x05040100u); }
__device__ __forceinline__ u32 phi(u32 a, u32 b) { return __builtin_amdgcn_perm(b, a, 0x07060302u); }
__device__ __forceinline__ u32 canon(u32 v) { return min(v, 0xFFFFFFFEu); }
__device__ __forceinline__ u32 max4(v4u t) { return max(max(t.x, t.y), max(t.z, t.w)); }
__device__ __forceinline__ u32 max5(u32 m, v4u t) { return max(max(max(m, t.x), t.y), max(t.z, t.w)); }
__device__ __forceinline__ bf8 asbf(v4u v) { return __builtin_bit_cast(bf8, v); }
__device__ __forceinline__ v2u trd(u32 a) { return __builtin_bit_cast(v2u, __builtin_amdgcn_ds_read_tr16_b64_v4i16(LDSP(s4, a))); }
template <int O> __device__ __forceinline__ v4u trd2(u32 a) { const v2u x = trd(a), y = trd(a + O); return v4u{x.x, x.y, y.x, y.y}; }
__device__ __forceinline__ void st128(v4u v, rsrc_t r, u32 off) { __builtin_amdgcn_raw_buffer_store_b128(v, r, off, 0, SC1); }
__device__ __forceinline__ void st64(u32 a, u32 b_, rsrc_t r, u32 off) { __builtin_amdgcn_raw_buffer_store_b64(v2u{a, b_}, r, off, 0, SC1); }
__device__ __forceinline__ v4u ld128(rsrc_t r, u32 off, u32 so) { return __builtin_amdgcn_raw_buffer_load_b128(r, off, so, SC1); }
__device__ __forceinline__ v2u ld64(rsrc_t r, u32 off, u32 so) { return __builtin_amdgcn_raw_buffer_load_b64(r, off, so, SC1); }
__device__ __forceinline__ u32 f2u(float v) { return __builtin_bit_cast(u32, v); }
__device__ __forceinline__ float u2f(u32 v) { return __builtin_bit_cast(float, v); }
__device__ __forceinline__ float opq(float v) { asm("" : "+v"(v)); return v; }
template <int CTRL> __device__ __forceinline__ float dppadd(float v) { return v + u2f(__builtin_amdgcn_update_dpp(0, f2u(v), CTRL, 0xF, 0xF, true)); }
__device__ __forceinline__ float swap16add(float a, float b) { const auto r = __builtin_amdgcn_permlane16_swap(f2u(a), f2u(b), false, false); return u2f(r[0]) + u2f(r[1]); }
__device__ __forceinline__ float swap32add(float a, float b) { const auto r = __builtin_amdgcn_permlane32_swap(f2u(a), f2u(b), false, false); return u2f(r[0]) + u2f(r[1]); }
__device__ __forceinline__ float wsum(float v) {
  v = dppadd<0xB1>(v); v = dppadd<0x4E>(v); v = dppadd<0x141>(v); v = dppadd<0x140>(v);
  v = swap16add(v, v);
  return swap32add(v, v);
}
template <u32 LI, u32 SO> __device__ __forceinline__ void dma16(u32 lb, u32 vo, v4u r) {
  u32 t;
  asm volatile("s_add_u32 m0, %1, %4\n\ts_mov_b32 %0, %5\n\tbuffer_load_dwordx4 %2, %3, %0 offen lds"
               : "=&s"(t) : "s"(lb), "v"(vo), "s"(r), "n"(LI), "n"(SO) : "memory", "scc");
}
__device__ __forceinline__ void warm(u32 vo, v4u r) {
  asm volatile("s_mov_b32 m0, %2\n\ts_nop 0\n\tbuffer_load_dword %0, %1, 0 offen lds" ::"v"(vo), "s"(r), "n"(L_DUM) : "memory");
}
template <int S0, int S1, int MS> __device__ __forceinline__ void dmaw(u32 lb, u32 vo, v4u r) {
  if constexpr (S0 < S1) { dma16<(S0 % 14) * 8192u + MS * 4096u, S0 * 57344u>(lb, vo, r); dmaw<S0 + 1, S1, MS>(lb, vo, r); }
}
template <int S0, int S1> __device__ __forceinline__ void dmaw2(u32 lb, u32 vo1, u32 vo3, v4u r1, v4u r3) {
  if constexpr (S0 < S1) { dma16<S0 * 8192u, S0 * 57344u>(lb, vo1, r1); dma16<S0 * 8192u + 4096u, S0 * 57344u>(lb, vo3, r3); dmaw2<S0 + 1, S1>(lb, vo1, vo3, r1, r3); }
}
template <int T> __device__ __forceinline__ void dmak(u32 lb, u32 vo, v4u r) {
  const u32 v1 = vo ^ 64u;
  dma16<0u, T * 28672u>(lb, vo, r); dma16<1024u, T * 28672u + 7168u>(lb, v1, r);
  dma16<2048u, T * 28672u + 14336u>(lb, vo, r); dma16<3072u, T * 28672u + 21504u>(lb, v1, r);
}
#define MFMA32(a, b, c) __builtin_amdgcn_mfma_f32_32x32x16_bf16(asbf(a), asbf(b), c, 0, 0, 0)
#define MFMA16(a, b, c) __builtin_amdgcn_mfma_f32_16x16x32_bf16(asbf(a), asbf(b), c, 0, 0, 0)
#define STAMP(i) do { if (i == 0) { asm volatile("s_memrealtime %0\n\ts_waitcnt lgkmcnt(0)" : "=s"(tw0)::"memory"); } if (i == TW_AT) { u64 t_; u32 n_ = 0; do { asm volatile("s_memrealtime %0\n\ts_waitcnt lgkmcnt(0)" : "=s"(t_)::"memory"); } while ((u32)(t_ - tw0) < TW_T && ++n_ < 100000u); } } while (0)
#define STOUT()
#define DLY1 0
#define DLY2 0
#define PSLP 4
#define HSLP 0
#define NE 2

template <int N, class SOF> __device__ __forceinline__ void settle(v4u* t, rsrc_t r, u32 off, SOF so) {
  u32 m = 0;
#pragma unroll
  for (int i = 0; i < N; i++) m = max5(m, t[i]);
  if (UNLIKELY(__any(m == 0xFFFFFFFFu))) {
#pragma nounroll
    for (u32 spin = 0; spin < SPIN_MAX; spin++) {
      bool any = false;
      FENCE();
#pragma unroll
      for (int i = 0; i < N; i++)
        if (__any(max4(t[i]) == 0xFFFFFFFFu)) { t[i] = ld128(r, off, so(i)); any = true; }
      if (!any) break;
    }
    DRAINV();
  }
}
__device__ __forceinline__ void waitflag(v2u f, rsrc_t r, u32 off, u32 t0, u32 t1) {
  if (UNLIKELY(__any((f.x != t0) | (f.y != t1)))) {
#pragma nounroll
    for (u32 spin = 0; spin < SPIN_MAX; spin++) {
      FENCE();
      f = ld64(r, off, 0);
      if (!__any((f.x != t0) | (f.y != t1))) break;
    }
    DRAINV();
  }
  FENCE();
}

__global__ void __launch_bounds__(512, 2)
fused_kernel(const u16* __restrict__ gx, const u16* __restrict__ gk, const u16* __restrict__ gv, const u16* __restrict__ gsk,
             const u16* __restrict__ gssm, const u16* __restrict__ gsd, const u16* __restrict__ gw1, const u16* __restrict__ gw3,
             const u16* __restrict__ gw2, u16* __restrict__ gout, unsigned char* __restrict__ ws) {
  asm volatile("" ::"s"(gx), "s"(gk), "s"(gv), "s"(gsk), "s"(gssm), "s"(gsd), "s"(gw1), "s"(gw3), "s"(gw2), "s"(gout), "s"(ws));
  int tid = threadIdx.x, lane = tid & 63;
  const int w = __builtin_amdgcn_readfirstlane(tid >> 6), b = blockIdx.x;
  const int xc = b & 7, ii = b >> 3;
  const bool mainwg = ii < 28;
  const int p = xc * 28 + ii;
  const int dt = ii % 7, q2 = xc & 1;
  const int rt = 4 * (xc >> 1) + ii / 7, fj = 7 * q2 + dt;
  const rsrc_t rws = mk(ws, WS_END);
  const v4u poison = {0xFFFFFFFFu, 0xFFFFFFFFu, 0xFFFFFFFFu, 0xFFFFFFFFu};
  u64 tw0;
  STAMP(0);

  u32 t0, t1;
  {
    const int pp = tid & 255;
    const u32 colo = pp < 224 ? (u32)(8 * b * 896 + pp * 4) : OOB;
    auto kvpart = [&](auto mw) {
      constexpr bool MW = decltype(mw)::value;
      u32 ra[11], rb[8];
      const rsrc_t rk = mk(gk, S * D * 2), rv = mk(gv, S * D * 2);
      const u32 halo = (b > 0 && pp < 224) ? colo - 3 * 896 : OOB;
#pragma unroll
      for (int i = 0; i < 3; i++) ra[i] = __builtin_amdgcn_raw_buffer_load_b32(rk, halo + i * 896, 0, 0);
#pragma unroll
      for (int i = 0; i < 8; i++) ra[3 + i] = __builtin_amdgcn_raw_buffer_load_b32(rk, colo + (i & 3) * 896, (i >> 2) * 3584, 0);
      const v4u skw = __builtin_amdgcn_raw_buffer_load_b128(mk(gsk, D * 8), pp * 16, 0, 0);
#pragma unroll
      for (int i = 0; i < 8; i++) rb[i] = __builtin_amdgcn_raw_buffer_load_b32(rv, colo + (i & 3) * 896, (i >> 2) * 3584, 0);
      const v2u ep = ld64(rws, WS_EPOCH, 0);
      FENCE();
      __builtin_amdgcn_s_barrier();
      FENCE();
      USE(skw);
      STAMP(1);
      v4u tp[4], cs[28];
      if constexpr (MW) {
        const rsrc_t rt_ = mk(gssm, D * S * 2);
        const u32 o = (u32)(2 * p * 4096 + pp * 16);
#pragma unroll
        for (int c = 0; c < 2; c++) {
          tp[2 * c] = __builtin_amdgcn_raw_buffer_load_b128(rt_, pp > 0 ? o - 16 : OOB, c * 4096, 0);
          tp[2 * c + 1] = __builtin_amdgcn_raw_buffer_load_b128(rt_, o, c * 4096, 0);
        }
      }
      FENCE();
      const float s0[4] = {lo2f(skw.x), hi2f(skw.x), lo2f(skw.y), hi2f(skw.y)};
      const float s1[4] = {lo2f(skw.z), hi2f(skw.z), lo2f(skw.w), hi2f(skw.w)};
      float k0[8], k1[8];
#pragma unroll
      for (int i = 0; i < 8; i++) {
        float a0 = lo2f(ra[i + 3]) * s0[0], a1 = hi2f(ra[i + 3]) * s1[0];
#pragma unroll
        for (int j = 1; j < 4; j++) { a0 += lo2f(ra[i + 3 - j]) * s0[j]; a1 += hi2f(ra[i + 3 - j]) * s1[j]; }
        k0[i] = a0 * lo2f(rb[i]); k1[i] = a1 * hi2f(rb[i]);
      }
      *LDSP(v4u, L_RX + pp * 32) = v4u{pk(k0[3], k0[2]), pk(k0[1], k0[0]), pk(k0[7], k0[6]), pk(k0[5], k0[4])};
      *LDSP(v4u, L_RX + pp * 32 + 16) = v4u{pk(k1[3], k1[2]), pk(k1[1], k1[0]), pk(k1[7], k1[6]), pk(k1[5], k1[4])};
      t0 = __builtin_amdgcn_readfirstlane(ep.x) + 1u; t1 = __builtin_amdgcn_readfirstlane(ep.y) ^ 0x5bd1e995u;
      FENCE();
      v2u pl[4];
#pragma unroll
      for (int i = 0; i < 4; i++) pl[i] = *LDSP(v2u, L_RX + w * 2048 + lane * 8 + i * 512);
      const u32 o = WS_KVG + b * 14336 + w * 4096 + lane * 16;
#pragma unroll
      for (int i = 0; i < 4; i++)
        if (i < 2 || w < 3) st128(v4u{pl[i].x, pl[i].y, t0, t1}, rws, o + i * 1024);
      FENCE();
      STAMP(2);
      BAR();
      STAMP(3);
      if constexpr (MW) {
#pragma unroll
        for (int i = 0; i < 4; i++) OPQ(tp[i]);
#pragma unroll
        for (int c = 0; c < 2; c++) {
          const u32 a[8] = {tp[2 * c].x, tp[2 * c].y, tp[2 * c].z, tp[2 * c].w, tp[2 * c + 1].x, tp[2 * c + 1].y, tp[2 * c + 1].z, tp[2 * c + 1].w};
          u32 od[7];
#pragma unroll
          for (int j = 0; j < 7; j++) od[j] = __builtin_amdgcn_alignbit(a[j + 1], a[j], 16);
          const u32 base = L_TAP + c * 16640 + (pp + 1) * 16;
          *LDSP(v4u, base) = v4u{od[0], od[1], od[2], od[3]};
          *LDSP(v4u, base + 4160) = v4u{a[1], a[2], a[3], a[4]};
          *LDSP(v4u, base + 8320) = v4u{od[1], od[2], od[3], od[4]};
          *LDSP(v4u, base + 12480) = v4u{a[2], a[3], a[4], a[5]};
          if (pp == 255) {
            *LDSP(v2u, base + 16) = v2u{od[4], od[5]};
            *LDSP(v2u, base + 16 + 4160) = v2u{a[5], a[6]};
            *LDSP(v2u, base + 16 + 8320) = v2u{od[5], od[6]};
            *LDSP(v2u, base + 16 + 12480) = v2u{a[6], a[7]};
          }
        }
        if (tid < 8) *LDSP(v4u, L_TAP + tid * 4160) = v4u{0u, 0u, 0u, 0u};
      } else {
        BAR();
        const int j = b - 224;
        const rsrc_t rw = mk(j < 16 ? gw1 : gw3, D * F * 2);
        const u32 o = pp < 224 ? (u32)((pp / 14) * 28 * 3584 + (j & 15) * 224 + (pp % 14) * 16) : OOB;
#pragma unroll
        for (int i = 0; i < 28; i++) cs[i] = __builtin_amdgcn_raw_buffer_load_b128(rw, o, i * 3584, 0);
        float sm[8];
#pragma unroll
        for (int e = 0; e < 8; e++) sm[e] = 0.f;
#pragma unroll
        for (int i = 0; i < 28; i++)
#pragma unroll
          for (int e = 0; e < 4; e++) { sm[2 * e] += lo2f(cs[i][e]); sm[2 * e + 1] += hi2f(cs[i][e]); }
        *LDSP(v4f, pp * 32) = v4f{sm[0], sm[1], sm[2], sm[3]};
        *LDSP(v4f, pp * 32 + 16) = v4f{sm[4], sm[5], sm[6], sm[7]};
      }
    };
    if (w < 4) {
      if (mainwg) kvpart(std::true_type{}); else kvpart(std::false_type{});
    } else {
      {
        const u32 o = mainwg ? WS_HT + (u32)((8 * (w - 4) + (lane >> 4)) * 57344 + p * 256 + (lane & 15) * 16) : OOB;
        st128(poison, rws, o);
        st128(poison, rws, o + 4 * 57344);
      }
      FENCE();
      const rsrc_t rx = mk(gx, S * D * 2);
      const u32 so = lane < 56 ? (u32)((8 * b + 2 * (w - 4)) * 896 + lane * 16) : OOB;
      v4u sr[2];
      sr[0] = __builtin_amdgcn_raw_buffer_load_b128(rx, so, 0, 0);
      sr[1] = __builtin_amdgcn_raw_buffer_load_b128(rx, so + 896, 0, 0);
      u32 rb[8];
#pragma unroll
      for (int i = 0; i < 8; i++) rb[i] = __builtin_amdgcn_raw_buffer_load_b32(rx, colo + (i & 3) * 896, (i >> 2) * 3584, 0);
      const v2u ep = ld64(rws, WS_EPOCH, 0);
      FENCE();
      WAITV(11);
      __builtin_amdgcn_s_barrier();
      FENCE();
      const v4u r1 = mkv(gw1, D * F * 2), r3 = mkv(gw3, D * F * 2);
      const u32 wb = L_W + (w - 4) * 1024;
      const u32 wrow = (u32)((4 * (w - 4) + (lane >> 4)) * 3584 + fj * 256);
      const u32 vo1 = wrow + (((lane & 15) ^ (2 * (lane >> 4))) * 16), vo3 = vo1 ^ 128;
      USE(ep); USE(rb[7]);
      STAMP(1);
      if (mainwg) dmaw2<0, NE>(wb, vo1, vo3, r1, r3);
      float mean[2], rstd[2];
#pragma unroll
      for (int r = 0; r < 2; r++) {
        const float e[8] = {lo2f(sr[r].x), hi2f(sr[r].x), lo2f(sr[r].y), hi2f(sr[r].y), lo2f(sr[r].z), hi2f(sr[r].z), lo2f(sr[r].w), hi2f(sr[r].w)};
        const float s = ((e[0] + e[1]) + (e[2] + e[3])) + ((e[4] + e[5]) + (e[6] + e[7]));
        mean[r] = wsum(s) * (1.f / D);
        float q = 0.f;
#pragma unroll
        for (int i = 0; i < 8; i++) { const float d = e[i] - mean[r]; q += d * d; }
        q = wsum(lane < 56 ? q : 0.f);
        rstd[r] = __builtin_amdgcn_rsqf(q * (1.f / D) + 1e-5f);
      }
      if (lane == 0) *LDSP(v4f, L_STAT + (w - 4) * 16) = v4f{mean[0], rstd[0], mean[1], rstd[1]};
      if (tid == 256) *LDSP(u32, L_CNT) = 0u;
      t0 = __builtin_amdgcn_readfirstlane(ep.x) + 1u; t1 = __builtin_amdgcn_readfirstlane(ep.y) ^ 0x5bd1e995u;
      STAMP(2);
      BAR();
      STAMP(3);
      v4f sa[4];
#pragma unroll
      for (int i = 0; i < 4; i++) sa[i] = *LDSP(v4f, L_STAT + i * 16);
      const u32 xb = L_W + 12 * 8192 + (w - 4) * 1024;
#pragma unroll
      for (int h = 0; h < 2; h++) {
        const v4f a = sa[2 * h], c = sa[2 * h + 1];
        const u32 *x = rb + 4 * h;
        *LDSP(v4u, xb + (lane >> 4) * 4096 + (lane & 15) * 64 + h * 16) =
            v4u{plo(x[0], x[1]), plo(x[2], x[3]), pk((lo2f(x[0]) - a.x) * a.y, (lo2f(x[1]) - a.z) * a.w), pk((lo2f(x[2]) - c.x) * c.y, (lo2f(x[3]) - c.z) * c.w)};
        *LDSP(v4u, xb + (lane >> 4) * 4096 + (lane & 15) * 64 + 32 + h * 16) =
            v4u{phi(x[0], x[1]), phi(x[2], x[3]), pk((hi2f(x[0]) - a.x) * a.y, (hi2f(x[1]) - a.z) * a.w), pk((hi2f(x[2]) - c.x) * c.y, (hi2f(x[3]) - c.z) * c.w)};
      }
      FENCE();
      v4u px[4];
#pragma unroll
      for (int i = 0; i < 4; i++) px[i] = *LDSP(v4u, xb + i * 4096 + lane * 16);
      const u32 o = WS_XQ + b * 14336 + (w - 4) * 4096 + lane * 16;
#pragma unroll
      for (int i = 0; i < 4; i++)
        if (i < 2 || w < 7) st128(px[i], rws, o + i * 1024);
      FENCE();
      {
        const u32 ho = mainwg ? WS_HF + (u32)((rt * 8 * 56 + 4 * fj + (w - 4)) * 1024 + lane * 16) : OOB;
#pragma unroll
        for (int i = 0; i < 8; i++) st128(poison, rws, ho + i * 56 * 1024);
      }
      WAITV(8);
      STAMP(4);
      if (lane == 0) {
        const u32 old = __hip_atomic_fetch_add(LDSP(u32, L_CNT), 1u, __ATOMIC_RELAXED, __HIP_MEMORY_SCOPE_WORKGROUP);
        if (old == 3u) st64(t0, t1, rws, WS_FLAG + b * 8);
      }
      FENCE();
      if (mainwg) dmaw2<NE, 14>(wb, vo1, vo3, r1, r3);
      if (!mainwg) BAR();
      STAMP(5);
    }
  }
  PHASE();
  if (!mainwg) {
    BAR();
    if (w > 0) {
      if (HSLP) __builtin_amdgcn_s_sleep(HSLP);
      const v4u rw2 = mkv(gw2, F * D * 2);
      const int q = tid - 64, r = q / 7;
      const u32 o = (u32)(((r >> 3) * 224 + (ii - 28) * 8 + (r & 7)) * 896 + (q % 7) * 128);
#pragma unroll
      for (int t = 0; t < 7; t++) warm(o + t * 28672, rw2);
      WAITV(0);
    }
    if (w == 0) {
      v2f s = {0.f, 0.f};
#pragma unroll
      for (int g = 0; g < 16; g++) s += *LDSP(v2f, g * 448 + lane * 8);
      st128(v4u{f2u(s.x), f2u(s.y), t0, t1}, rws, lane < 56 ? WS_CS + (b - 224) * 896 + lane * 16 : OOB);
    }
    STAMP(23);
    STOUT();
    return;
  }

  if (w < 4) {
    STAMP(4);
    if (DLY1) __builtin_amdgcn_s_sleep(DLY1);
    v4u g[4];
    const u32 o = WS_KVG + (tid >> 2) * 14336 + p * 64 + (tid & 3) * 16;
#pragma unroll
    for (int i = 0; i < 4; i++) g[i] = ld128(rws, o, i * 64 * 14336);
#pragma nounroll
    for (u32 spin = 0; spin < SPIN_MAX; spin++) {
      bool bad[4], any = false;
#pragma unroll
      for (int i = 0; i < 4; i++) { bad[i] = (g[i].z != t0) | (g[i].w != t1); any |= bad[i]; }
      if (!__any(any)) break;
      __builtin_amdgcn_s_sleep(PSLP);
      FENCE();
#pragma unroll
      for (int i = 0; i < 4; i++) if (bad[i]) g[i] = ld128(rws, o, i * 64 * 14336);
    }
    STAMP(5);
    const u32 la = L_RX + ((tid >> 1) & 1) * 4608 + (508 - 8 * (tid >> 2) - 4 * (tid & 1)) * 2;
#pragma unroll
    for (int i = 0; i < 4; i++) *LDSP(v2u, la + (3 - i) * 1024) = v2u{g[i].x, g[i].y};
    if (tid < 64) *LDSP(v4u, L_RX + (tid >> 5) * 4608 + 4096 + (tid & 31) * 16) = v4u{0u, 0u, 0u, 0u};
  }
  const int ch = w >> 2, wq = w & 3;
  const float dval = lo2f(gsd[2 * p + ch]);
  const u32 fo1 = WS_FLAG + (64 * wq + lane) * 8;
  BAR();
  STAMP(6);
  v4u w13[28];
  const u32 wra = L_W + (((lane >> 4) & 1) * 4 + 2 * (lane >> 5)) * 1024 + ((lane & 15) >> 2) * 256 +
                  (((2 * w + (lane & 1)) ^ (2 * ((lane & 15) >> 2)) ^ (8 * ((lane >> 4) & 1))) * 16) + ((lane >> 1) & 1) * 8;
  {
    const v2u f1 = ld64(rws, fo1, 0);
    FENCE();
    const int la_ = lane & 15, kg = lane >> 4;
    v4f acc[8];
#pragma unroll
    for (int n = 0; n < 8; n++) acc[n] = v4f{0.f, 0.f, 0.f, 0.f};
    u32 aa = L_TAP + ch * 16640 + (la_ & 3) * 4160 + ((la_ >> 2) + 2 * kg + 16 * wq) * 8, ab = aa + 8;
    asm volatile("" : "+v"(ab));
    const u32 ba = L_RX + ch * 4608 + (30 + 8 * wq - 2 * la_ + kg) * 16;
    auto lda = [&](int o) { const v2u x = *LDSP(v2u, aa + o), y = *LDSP(v2u, ab + o); return v4u{x.x, x.y, y.x, y.y}; };
    v4u A[8], B[8], A2[8], B2[8];
#pragma unroll
    for (int m = 0; m < 8; m++) { A[m] = lda(m * 512); B[m] = *LDSP(v4u, ba + (7 - m) * 512); SCHED(); }
#pragma unroll
    for (int g = 0; g < 8; g++) {
      A2[g] = lda(64 + g * 512); B2[g] = *LDSP(v4u, ba + 64 + (7 - g) * 512);
#pragma unroll
      for (int m = 0; m <= g; m++) {
        if (m + g < 8) acc[m + g] = MFMA16(A[m], B[g], acc[m + g]);
        if (m < g && m + g < 8) acc[m + g] = MFMA16(A[g], B[m], acc[m + g]);
      }
      SCHED();
    }
    const int t = 512 * wq + 16 * la_ + 4 * kg;
    const v2u kv0 = *LDSP(v2u, L_RX + ch * 4608 + (2044 - t) * 2), kv1 = *LDSP(v2u, L_RX + ch * 4608 + (2044 - 256 - t) * 2);
    waitflag(f1, rws, fo1, t0, t1);
    WAITV(0);
    STAMP(7);
    BAR();
    STAMP(8);
    const u32 xqo = WS_XQ + (64 * wq + 2 * la_ + (kg >> 1)) * 14336 + p * 64 + ch * 32 + (kg & 1) * 16;
    const v4u xq0 = ld128(rws, xqo, 0), xq1 = ld128(rws, xqo, 32 * 14336);
    FENCE();
#pragma unroll
    for (int d = 7; d >= 0; d--) {
      if (d > 0) { w13[14 - 2 * d] = trd2<1024>(wra + (14 - 2 * d) * 8192); w13[15 - 2 * d] = trd2<1024>(wra + (15 - 2 * d) * 8192); }
#pragma unroll
      for (int m = 0; m <= d; m++) acc[d] = MFMA16(A2[m], B2[d - m], acc[d]);
      SCHED();
    }
#pragma unroll
    for (int n = 7; n >= 0; n--)
      if ((n >> 1) != wq) *LDSP(v4f, L_RED + (w * 6 + (n < 2 * wq ? n : n - 2)) * 1024 + lane * 16) = acc[n];
    v4f y0, y1;
#pragma unroll
    for (int n = 0; n < 4; n++) if (n == wq) { y0 = acc[2 * n]; y1 = acc[2 * n + 1]; }
    BAR();
    STAMP(9);
#pragma unroll
    for (int sq = 1; sq < 4; sq++) {
      const int sw = (wq + sq) & 3;
      const u32 a = L_RED + ((ch * 4 + sw) * 6 + (wq < sw ? 2 * wq : 2 * wq - 2)) * 1024 + lane * 16;
      y0 += *LDSP(v4f, a);
      y1 += *LDSP(v4f, a + 1024);
    }
    const u32 h0x = pk(lo2f(xq0.x) + lo2f(xq0.z) * (y0.x + hi2f(kv0.y) * dval), hi2f(xq0.x) + hi2f(xq0.z) * (y0.y + lo2f(kv0.y) * dval));
    const u32 h0y = pk(lo2f(xq0.y) + lo2f(xq0.w) * (y0.z + hi2f(kv0.x) * dval), hi2f(xq0.y) + hi2f(xq0.w) * (y0.w + lo2f(kv0.x) * dval));
    const u32 h1x = pk(lo2f(xq1.x) + lo2f(xq1.z) * (y1.x + hi2f(kv1.y) * dval), hi2f(xq1.x) + hi2f(xq1.z) * (y1.y + lo2f(kv1.y) * dval));
    const u32 h1y = pk(lo2f(xq1.y) + lo2f(xq1.w) * (y1.z + hi2f(kv1.x) * dval), hi2f(xq1.y) + hi2f(xq1.w) * (y1.w + lo2f(kv1.x) * dval));
    const int s1 = (wq + 1) & 3;
    const u32 tb = L_RED + ((ch * 4 + s1) * 6 + (wq < s1 ? 2 * wq : 2 * wq - 2)) * 1024;
    *LDSP(v2u, tb + la_ * 32 + kg * 8) = v2u{canon(h0x), canon(h0y)};
    *LDSP(v2u, tb + 512 + la_ * 32 + kg * 8) = v2u{canon(h1x), canon(h1y)};
    FENCE();
    st128(*LDSP(v4u, tb + lane * 16), rws, WS_HT + (u32)((8 * wq + (lane >> 3)) * 57344 + (2 * p + ch) * 128 + (lane & 7) * 16));
  }
  FENCE();
  STAMP(10);
  PHASE();

  {
    const v4u rw = mkv(w < 4 ? gw1 : gw3, D * F * 2);
    const u32 vo = (u32)((4 * wq + (lane >> 4)) * 3584 + fj * 256) + (((lane & 15) ^ (2 * (lane >> 4)) ^ (8 * ch)) * 16);
    dmaw<14, 28, 0>(L_W + w * 1024, vo, rw);
  }
  int hh = lane >> 5;
  u32 hfo = WS_HF + (u32)(((rt * 2 + ((lane >> 4) & 1)) * 4 * 56 + 4 * fj + (w >> 1)) * 1024 + ((2 * (w & 1) + hh) * 16 + (lane & 15)) * 16);
  OPQ(hfo);
  FENCE();
  if (DLY2) __builtin_amdgcn_s_sleep(DLY2);
  v4u hc[14];
  const u32 hto = WS_HT + rt * 114688 - 8192 + (tid >> 2) * 128 + (tid & 3) * 16;
  const u32 so3 = w < 4 ? 57344u : 64u;
  auto hso = [&](int i) -> u32 { const int j = i % 7; return (i / 7) * 57344u + (j < 3 ? 8192u + j * 16384u : j == 3 ? so3 : 64u + 16384u * (j - 3)); };
#pragma unroll
  for (int i = 0; i < 4; i++) hc[i] = ld128(rws, hto, hso(i));
  FENCE();
  settle<4>(hc, rws, hto, hso);
  WAITV(0);
#pragma unroll
  for (int i = 4; i < 14; i++) hc[i] = ld128(rws, hto, hso(i));
  const u32 cso = w >= 6 ? WS_CS + (u32)((w - 6) * 14336 + fj * 1024 + lane * 16) : OOB;
  v4u csl = ld128(rws, cso, 0);
  FENCE();
  STAMP(11);
  BAR();
#pragma unroll
  for (int s = 14; s < 28; s++) w13[s] = trd2<1024>(wra + (s - 14) * 8192);
#pragma unroll
  for (int i = 0; i < 3; i++) *LDSP(v4u, L_Q0 + tid * 16 + i * 8192) = hc[i];
  if (w < 4) *LDSP(v4u, L_Q0 + tid * 16 + 24576) = hc[3];
  BAR();
  STAMP(12);
  const v4u ones = {0x3F803F80u, 0x3F803F80u, 0x3F803F80u, 0x3F803F80u};
  const u32 trb = (8 * hh + ((lane & 15) >> 2)) * 64 + ((lane >> 4) & 1) * 32 + (lane & 3) * 8;
  const u32 lw = L_Q1 + 4096 + tid * 16;
  auto stats = [&](auto ns, int rg, int s0, int slot) {
    constexpr int NS = decltype(ns)::value;
    const u32 sb = (rg < 2 ? L_Q0 : L_Q1 + ((rg >> 1) - 1) * 28672) + s0 * 2048 + (8 * (lane >> 4) + ((lane & 15) >> 2)) * 64 + (rg & 1) * 32 + (lane & 3) * 8;
    v4f g = {0.f, 0.f, 0.f, 0.f}, sm = {0.f, 0.f, 0.f, 0.f};
    v4u fr[NS];
#pragma unroll
    for (int s = 0; s < NS; s++) fr[s] = trd2<256>(sb + s * 2048);
#pragma unroll
    for (int s = 0; s < NS; s++) { g = MFMA16(fr[s], fr[s], g); sm = MFMA16(ones, fr[s], sm); }
    const int i = lane & 3;
    const float sq = i == 0 ? g.x : i == 1 ? g.y : i == 2 ? g.z : g.w;
    if (((lane & 15) >> 2) == (lane >> 4)) {
      const u32 a = L_ST + (16 * rg + (lane & 15)) * 16;
      if (NS == 14) *LDSP(v4f, a) = v4f{sm.x, sq, 0.f, 0.f}; else *LDSP(v2f, a + slot * 8) = v2f{sm.x, sq};
    }
  };
  float nm, ny, cv;
  auto getst = [&](int nt) {
    const v4f s = *LDSP(v4f, L_ST + (32 * nt + (lane & 31)) * 16);
    nm = opq(s.x + s.z) * (-1.f / D);
    cv = fmaxf(__builtin_fmaf(-nm, nm, opq(s.y + s.w) * (1.f / D)), 0.f) + 1e-5f;
    ny = -1.4426950408889634f * __builtin_amdgcn_rsqf(cv);
  };
  auto epi = [&](float a, float u, float c1, float c3) -> float {
    const float g = opq(__builtin_fmaf(nm, c1, a)), uu = opq(__builtin_fmaf(nm, c3, u));
    const float e = __builtin_amdgcn_exp2f(opq(g * ny));
    return opq(opq(g * uu) * __builtin_amdgcn_rcpf(opq(__builtin_fmaf(e, cv, cv))));
  };
  if (w < 4) stats(std::integral_constant<int, 7>{}, w >> 1, 7 * (w & 1), w & 1);
  SCHED();
  const v4u rw2 = mkv(gw2, F * D * 2);
  u32 w2o = (u32)((224 * w + (lane >> 3)) * 896 + dt * 128 + ((lane & 7) ^ ((lane >> 3) & 2)) * 16);
  u32 w2a = (u32)((lane >> 4) * 1024 + ((lane & 15) >> 2) * 128 + (((lane >> 3) & 1) | ((lane >> 3) & 2)) * 32 + (lane & 3) * 8);
  OPQ(w2o); OPQ(w2a);
  const u32 slA = w < 7 ? 86016u + w * 4096u : 147456u, slB = w < 7 ? L_Q0 + w * 4096u : 151552u, slC = w < 7 ? L_Q1 + w * 4096u : 155648u;
  v4u wa[7][4];
  auto rdw = [&](int t, u32 sl) {
    const u32 a = w2a + sl;
#pragma unroll
    for (int j = 0; j < 4; j++) wa[t][j] = trd2<512>(a ^ (32u * j));
  };
  auto usew = [&](int t) { asm volatile("" ::"v"(wa[t][0]), "v"(wa[t][1]), "v"(wa[t][2]), "v"(wa[t][3])); };
  auto rdres = [&]() {
    return trd2<256>(((w >> 1) == 0 ? L_Q0 : L_Q1 + ((w >> 1) - 1) * 28672) + (64 * dt + 16 * (lane >> 4) + 8 * ((lane >> 1) & 1) + ((lane & 15) >> 2)) * 64 + 32 * q2 + 16 * (w & 1) + 8 * (lane & 1));
  };
  const u32 h2o = WS_HF + (u32)(((rt * 2 + q2) * 4 * 56 + 7 * w) * 1024 + lane * 16);
  const u32 csa = L_CSV + (16 * w + 8 * hh) * 4;
  auto lin = [](int i) -> u32 { return i * 1024u; };
  v4u hf[4][7], res;
  v16f ac[4];
#define PDX 4
  constexpr int PD = PDX;
#pragma unroll
  for (int nt = 0; nt < 4; nt++) {
    u32 tb_ = trb + (nt == 0 ? L_Q0 : L_Q1 + (nt - 1) * 28672);
    OPQ(tb_);
    v4u bfr[28];
#pragma unroll
    for (int s = 0; s < PD; s++) bfr[s] = trd2<256>(tb_ + s * 1024);
    float hv[8];
    v4f c1, c3;
    if (nt > 0) getst(nt - 1);
#pragma unroll
    for (int r = 0; r < 16; r++) ac[nt][r] = 0.f;
    SCHED();
#pragma unroll
    for (int s = 0; s < 28; s++) {
      if (s + PD < 28) bfr[s + PD] = trd2<256>(tb_ + (s + PD) * 1024);
      if (nt > 0 && (s == 0 || s == 9)) { c1 = *LDSP(v4f, csa + (s ? 16 : 0)); c3 = *LDSP(v4f, csa + 512 + (s ? 16 : 0)); }
      ac[nt] = MFMA32(w13[s], bfr[s], ac[nt]);
      if (nt == 0) {
        if (s == 8) settle<3>(hc + 4, rws, hto, [&](int i) { return hso(i + 4); });
        if (s == 7 && w >= 4) *LDSP(v4u, L_Q1 + (tid & 255) * 16) = hc[3];
        if (s >= 8 && s < 11) *LDSP(v4u, lw + (s - 8) * 8192) = hc[s - 4];
        if (s == 15) settle<4>(hc + 7, rws, hto, [&](int i) { return hso(i + 7); });
        if (s >= 15 && s < 19) *LDSP(v4u, lw + (s - 12) * 8192) = hc[s - 8];
        if (s == 22) settle<3>(hc + 11, rws, hto, [&](int i) { return hso(i + 11); });
        if (s >= 22 && s < 25) *LDSP(v4u, lw + (s - 15) * 8192) = hc[s - 11];
      } else {
        if (s >= 2 && s < 18 && (s & 1) == 0) { const int r = (s - 2) >> 1; hv[r] = epi(ac[nt - 1][r], ac[nt - 1][r + 8], c1[r & 3], c3[r & 3]); }
        if (s == 18) st128(v4u{canon(pk(hv[0], hv[1])), canon(pk(hv[2], hv[3])), canon(pk(hv[4], hv[5])), canon(pk(hv[6], hv[7]))}, rws, hfo);
        if (s == 19) { hfo += 56 * 1024; OPQ(hfo); }
      }
      if (nt == 0 && s == 25) {
        if (w >= 6) {
          if (UNLIKELY(__any((csl.z != t0) | (csl.w != t1)))) {
#pragma nounroll
            for (u32 spin = 0; spin < SPIN_MAX; spin++) {
              FENCE();
              csl = ld128(rws, cso, 0);
              if (!__any((csl.z != t0) | (csl.w != t1))) break;
            }
            DRAINV();
          }
          *LDSP(v2u, L_CSV + (tid - 384) * 8) = v2u{csl.x, csl.y};
        }
        dmak<0>(slA, w2o, rw2);
      }
      if (nt == 1) {
        if (s == 0) dmak<1>(slB, w2o, rw2);
        if (s == 6) { WAITV(4); rdw(0, slA); }
        if (s == 9) { usew(0); dmak<2>(slA, w2o, rw2); }
        if (s == 16) { WAITV(4); rdw(1, slB); }
        if (s == 19) { usew(1); dmak<3>(slB, w2o, rw2); }
      }
      if (nt == 2) {
        if (s == 0) dmak<4>(slC, w2o, rw2);
        if (s == 4) { WAITV(9); rdw(2, slA); }
        if (s == 7) { usew(2); dmak<5>(slA, w2o, rw2); }
      }
      if (nt == 3) {
        if (s == 4) { WAITV(1); rdw(5, slA); }
        if (s == 7) { usew(5); dmak<6>(slA, w2o, rw2); }
        if (s == 8) rdw(3, slB);
        if (s == 12) rdw(4, slC);
        if (s >= 13 && s < 20) hf[0][s - 13] = ld128(rws, h2o, (s - 13) * 1024);
        if (s >= 20 && s < 27) hf[1][s - 20] = ld128(rws, h2o, (56 + s - 20) * 1024);
      }
      SCHED();
    }
    STAMP(13 + nt);
    if (nt == 0) {
      if (w < 2) res = rdres();
      BAR();
      if (w >= 2) res = rdres();
      if (w < 4) stats(std::integral_constant<int, 7>{}, 2 + (w >> 1), 7 * (w & 1), w & 1);
      else stats(std::integral_constant<int, 14>{}, w, 0, 0);
    }
    if (nt == 1) BAR();
    SCHED();
  }
  {
    getst(3);
    float hv[8];
#pragma unroll
    for (int h = 0; h < 2; h++) {
      const v4f c1 = *LDSP(v4f, csa + 16 * h), c3 = *LDSP(v4f, csa + 512 + 16 * h);
#pragma unroll
      for (int r = 0; r < 4; r++) hv[4 * h + r] = epi(ac[3][4 * h + r], ac[3][4 * h + r + 8], c1[r], c3[r]);
    }
    st128(v4u{canon(pk(hv[0], hv[1])), canon(pk(hv[2], hv[3])), canon(pk(hv[4], hv[5])), canon(pk(hv[6], hv[7]))}, rws, hfo);
  }
  FENCE();
#pragma unroll
  for (int s = 0; s < 7; s++) hf[2][s] = ld128(rws, h2o, (2 * 56 + s) * 1024);
  FENCE();
  WAITV(23);
  rdw(6, slA);
  STAMP(17);
  PHASE();
  const u32 pw = (u32)(w * 4096 + lane * 16);
  auto finalize = [&]() {
    const int n = 8 * (w & 1) + (lane & 7);
    const u32 a = (u32)((w >> 1) * 32768 + (lane >> 3) * 512 + (n ^ ((lane >> 1) & 8)) * 16);
    v4f e = *LDSP(v4f, a), o = *LDSP(v4f, a + 256);
#pragma unroll
    for (int u = 1; u < 8; u++) { e += *LDSP(v4f, a + u * 4096); o += *LDSP(v4f, a + u * 4096 + 256); }
    const v4u ov = {pk(lo2f(res.x) + e.x, hi2f(res.x) + e.y), pk(lo2f(res.y) + e.z, hi2f(res.y) + e.w),
                    pk(lo2f(res.z) + o.x, hi2f(res.z) + o.y), pk(lo2f(res.w) + o.z, hi2f(res.w) + o.w)};
    *(v4u*)(gout + (128 * rt + 32 * (w >> 1) + 16 * q2 + n) * D + 64 * dt + 8 * (lane >> 3)) = ov;
  };
#pragma unroll
  for (int g = 0; g < 4; g++) {
    if (g == 3) {
      BAR();
      STAMP(22);
      if (w < 6) finalize();
      FENCE();
    }
    settle<7>(hf[g], rws, h2o + g * 56 * 1024, lin);
    STAMP(18 + g);
    v4f c[4];
#pragma unroll
    for (int i = 0; i < 4; i++) c[i] = v4f{0.f, 0.f, 0.f, 0.f};
#pragma unroll
    for (int s = 0; s < 7; s++)
#pragma unroll
      for (int i = 0; i < 4; i++) c[i] = MFMA16(wa[s][i], hf[g][s], c[i]);
    SCHED();
    if (g == 0) BAR();
    if (g == 1) {
#pragma unroll
      for (int s = 0; s < 7; s++) hf[3][s] = ld128(rws, h2o, (3 * 56 + s) * 1024);
      FENCE();
    }
#pragma unroll
    for (int i = 0; i < 4; i++) *LDSP(v4f, g * 32768 + (pw ^ (128u * (i & 1))) + i * 1024) = c[i];
    SCHED();
  }
  BAR();
  if (w >= 6) finalize();
  if (b == 0 && tid == 0) st64(t0, t1 ^ 0x5bd1e995u, rws, WS_EPOCH);
  STAMP(23);
  STOUT();
}

extern "C" void kernel_launch(void* const* d_in, const int* in_sizes, int n_in, void* d_out, int out_size, void* d_ws, size_t ws_size,
                              hipStream_t stream) {
  (void)hipFuncSetAttribute((const void*)fused_kernel, hipFuncAttributeMaxDynamicSharedMemorySize, L_TOTAL);
  fused_kernel<<<256, 512, L_TOTAL, stream>>>((const u16*)d_in[0], (const u16*)d_in[1], (const u16*)d_in[2], (const u16*)d_in[3],
                                              (const u16*)d_in[4], (const u16*)d_in[5], (const u16*)d_in[7], (const u16*)d_in[8],
                                              (const u16*)d_in[9], (u16*)d_out, (unsigned char*)d_ws);
}
```

```cpp
#include <hip/hip_runtime.h>
#include <hip/hip_bf16.h>
#include <type_traits>

typedef unsigned u32;
typedef unsigned short u16;
typedef unsigned long long u64;
typedef u32 v4u __attribute__((ext_vector_type(4)));
typedef u32 v2u __attribute__((ext_vector_type(2)));
typedef float v4f __attribute__((ext_vector_type(4)));
typedef float v2f __attribute__((ext_vector_type(2)));
typedef float v16f __attribute__((ext_vector_type(16)));
typedef __bf16 bf8 __attribute__((ext_vector_type(8)));
typedef __bf16 bf2 __attribute__((ext_vector_type(2)));
typedef short s4 __attribute__((ext_vector_type(4)));
typedef __amdgpu_buffer_rsrc_t rsrc_t;
#define LDSP(T, off) ((__attribute__((address_space(3))) T*)(size_t)(off))

constexpr int S = 2048, D = 448, F = 1792;
constexpr u32 WS_EPOCH = 0;
constexpr u32 WS_FLAG = 4096;
constexpr u32 WS_CS = 16384;
constexpr u32 WS_KVG = 49152;
constexpr u32 WS_XQ = WS_KVG + 256u * 224 * 64;
constexpr u32 WS_HT = WS_XQ + 256u * 224 * 64;
constexpr u32 WS_HF = WS_HT + 32u * 448 * 128;
constexpr u32 WS_END = WS_HF + 128u * 56 * 1024;
constexpr u32 WS_DBG = WS_END;
constexpr u32 L_W = 0;
constexpr u32 L_TAP = 114688;
constexpr u32 L_RX = 147968;
constexpr u32 L_DUM = 157184;
constexpr u32 L_STAT = 162816, L_CNT = L_STAT + 64;
constexpr u32 L_RED = 114688;
constexpr u32 L_Q0 = 114688, L_Q1 = 0;
constexpr u32 L_ST = 143360;
constexpr u32 L_CSV = 145408;
constexpr u32 L_TOTAL = 160 * 1024;
constexpr u32 OOB = 0x80000000u;
constexpr int SC1 = 16;
constexpr u32 SPIN_MAX = 1u << 16;

#define FENCE() asm volatile("" ::: "memory")
#define WAITV(n) asm volatile("s_waitcnt vmcnt(" #n ")" ::: "memory")
#define DRAINV() __builtin_amdgcn_s_waitcnt(0x0F70)
#define BAR() do { asm volatile("s_waitcnt lgkmcnt(0)" ::: "memory"); __builtin_amdgcn_s_barrier(); FENCE(); } while (0)
#define SCHED() __builtin_amdgcn_sched_barrier(0)
#define PHASE() do { SCHED(); asm volatile("" : "+v"(tid)); lane = tid & 63; SCHED(); } while (0)
#define UNLIKELY(x) __builtin_expect(!!(x), 0)
#define USE(x) asm volatile("" ::"v"(x))
#define OPQ(x) asm volatile("" : "+v"(x))

__device__ __forceinline__ rsrc_t mk(const void* p, u32 bytes) { return __builtin_amdgcn_make_buffer_rsrc((void*)p, 0, bytes, 0x00020000); }
__device__ __forceinline__ v4u mkv(const void* p, u32 bytes) { const u64 a = (u64)p; return v4u{(u32)a, (u32)(a >> 32) & 0xffffu, bytes, 0x00020000u}; }
__device__ __forceinline__ float lo2f(u32 v) { return __builtin_bit_cast(float, v << 16); }
__device__ __forceinline__ float hi2f(u32 v) { return __builtin_bit_cast(float, v & 0xffff0000u); }
__device__ __forceinline__ u32 pk(float a, float b) { return __builtin_bit_cast(u32, __builtin_convertvector(v2f{a, b}, bf2)); }
__device__ __forceinline__ u32 plo(u32 a, u32 b) { return __builtin_amdgcn_perm(b, a, 0x05040100u); }
__device__ __forceinline__ u32 phi(u32 a, u32 b) { return __builtin_amdgcn_perm(b, a, 0x07060302u); }
__device__ __forceinline__ u32 canon(u32 v) { return min(v, 0xFFFFFFFEu); }
__device__ __forceinline__ u32 max4(v4u t) { return max(max(t.x, t.y), max(t.z, t.w)); }
__device__ __forceinline__ u32 max5(u32 m, v4u t) { return max(max(max(m, t.x), t.y), max(t.z, t.w)); }
__device__ __forceinline__ bf8 asbf(v4u v) { return __builtin_bit_cast(bf8, v); }
__device__ __forceinline__ v2u trd(u32 a) { return __builtin_bit_cast(v2u, __builtin_amdgcn_ds_read_tr16_b64_v4i16(LDSP(s4, a))); }
template <int O> __device__ __forceinline__ v4u trd2(u32 a) { const v2u x = trd(a), y = trd(a + O); return v4u{x.x, x.y, y.x, y.y}; }
__device__ __forceinline__ void st128(v4u v, rsrc_t r, u32 off) { __builtin_amdgcn_raw_buffer_store_b128(v, r, off, 0, SC1); }
__device__ __forceinline__ void st64(u32 a, u32 b_, rsrc_t r, u32 off) { __builtin_amdgcn_raw_buffer_store_b64(v2u{a, b_}, r, off, 0, SC1); }
__device__ __forceinline__ v4u ld128(rsrc_t r, u32 off, u32 so) { return __builtin_amdgcn_raw_buffer_load_b128(r, off, so, SC1); }
__device__ __forceinline__ v2u ld64(rsrc_t r, u32 off, u32 so) { return __builtin_amdgcn_raw_buffer_load_b64(r, off, so, SC1); }
__device__ __forceinline__ u32 f2u(float v) { return __builtin_bit_cast(u32, v); }
__device__ __forceinline__ float u2f(u32 v) { return __builtin_bit_cast(float, v); }
__device__ __forceinline__ float opq(float v) { asm("" : "+v"(v)); return v; }
template <int CTRL> __device__ __forceinline__ float dppadd(float v) { return v + u2f(__builtin_amdgcn_update_dpp(0, f2u(v), CTRL, 0xF, 0xF, true)); }
__device__ __forceinline__ float swap16add(float a, float b) { const auto r = __builtin_amdgcn_permlane16_swap(f2u(a), f2u(b), false, false); return u2f(r[0]) + u2f(r[1]); }
__device__ __forceinline__ float swap32add(float a, float b) { const auto r = __builtin_amdgcn_permlane32_swap(f2u(a), f2u(b), false, false); return u2f(r[0]) + u2f(r[1]); }
__device__ __forceinline__ float wsum(float v) {
  v = dppadd<0xB1>(v); v = dppadd<0x4E>(v); v = dppadd<0x141>(v); v = dppadd<0x140>(v);
  v = swap16add(v, v);
  return swap32add(v, v);
}
template <u32 LI, u32 SO> __device__ __forceinline__ void dma16(u32 lb, u32 vo, v4u r) {
  u32 t;
  asm volatile("s_add_u32 m0, %1, %4\n\ts_mov_b32 %0, %5\n\tbuffer_load_dwordx4 %2, %3, %0 offen lds"
               : "=&s"(t) : "s"(lb), "v"(vo), "s"(r), "n"(LI), "n"(SO) : "memory", "scc");
}
__device__ __forceinline__ void warm(u32 vo, v4u r) {
  asm volatile("s_mov_b32 m0, %2\n\ts_nop 0\n\tbuffer_load_dword %0, %1, 0 offen lds" ::"v"(vo), "s"(r), "n"(L_DUM) : "memory");
}
template <int S0, int S1, int MS> __device__ __forceinline__ void dmaw(u32 lb, u32 vo, v4u r) {
  if constexpr (S0 < S1) { dma16<(S0 % 14) * 8192u + MS * 4096u, S0 * 57344u>(lb, vo, r); dmaw<S0 + 1, S1, MS>(lb, vo, r); }
}
template <int S0, int S1> __device__ __forceinline__ void dmaw2(u32 lb, u32 vo1, u32 vo3, v4u r1, v4u r3) {
  if constexpr (S0 < S1) { dma16<S0 * 8192u, S0 * 57344u>(lb, vo1, r1); dma16<S0 * 8192u + 4096u, S0 * 57344u>(lb, vo3, r3); dmaw2<S0 + 1, S1>(lb, vo1, vo3, r1, r3); }
}
template <int T> __device__ __forceinline__ void dmak(u32 lb, u32 vo, v4u r) {
  const u32 v1 = vo ^ 64u;
  dma16<0u, T * 28672u>(lb, vo, r); dma16<1024u, T * 28672u + 7168u>(lb, v1, r);
  dma16<2048u, T * 28672u + 14336u>(lb, vo, r); dma16<3072u, T * 28672u + 21504u>(lb, v1, r);
}
#define MFMA32(a, b, c) __builtin_amdgcn_mfma_f32_32x32x16_bf16(asbf(a), asbf(b), c, 0, 0, 0)
#define MFMA16(a, b, c) __builtin_amdgcn_mfma_f32_16x16x32_bf16(asbf(a), asbf(b), c, 0, 0, 0)
#define STAMP(i)
#define STOUT()
#define DLY1 0
#define DLY2 0
#define PSLP 4
#define HSLP 0
#define NE 2

template <int N, class SOF> __device__ __forceinline__ void settle(v4u* t, rsrc_t r, u32 off, SOF so) {
  u32 m = 0;
#pragma unroll
  for (int i = 0; i < N; i++) m = max5(m, t[i]);
  if (UNLIKELY(__any(m == 0xFFFFFFFFu))) {
#pragma nounroll
    for (u32 spin = 0; spin < SPIN_MAX; spin++) {
      bool any = false;
      FENCE();
#pragma unroll
      for (int i = 0; i < N; i++)
        if (__any(max4(t[i]) == 0xFFFFFFFFu)) { t[i] = ld128(r, off, so(i)); any = true; }
      if (!any) break;
    }
    DRAINV();
  }
}
__device__ __forceinline__ void waitflag(v2u f, rsrc_t r, u32 off, u32 t0, u32 t1) {
  if (UNLIKELY(__any((f.x != t0) | (f.y != t1)))) {
#pragma nounroll
    for (u32 spin = 0; spin < SPIN_MAX; spin++) {
      FENCE();
      f = ld64(r, off, 0);
      if (!__any((f.x != t0) | (f.y != t1))) break;
    }
    DRAINV();
  }
  FENCE();
}

__global__ void __launch_bounds__(512, 2)
fused_kernel(const u16* __restrict__ gx, const u16* __restrict__ gk, const u16* __restrict__ gv, const u16* __restrict__ gsk,
             const u16* __restrict__ gssm, const u16* __restrict__ gsd, const u16* __restrict__ gw1, const u16* __restrict__ gw3,
             const u16* __restrict__ gw2, u16* __restrict__ gout, unsigned char* __restrict__ ws) {
  asm volatile("" ::"s"(gx), "s"(gk), "s"(gv), "s"(gsk), "s"(gssm), "s"(gsd), "s"(gw1), "s"(gw3), "s"(gw2), "s"(gout), "s"(ws));
  int tid = threadIdx.x, lane = tid & 63;
  const int w = __builtin_amdgcn_readfirstlane(tid >> 6), b = blockIdx.x;
  const int xc = b & 7, ii = b >> 3;
  const bool mainwg = ii < 28;
  const int p = xc * 28 + ii;
  const int dt = ii % 7, q2 = xc & 1;
  const int rt = 4 * (xc >> 1) + ii / 7, fj = 7 * q2 + dt;
  const rsrc_t rws = mk(ws, WS_END);
  const v4u poison = {0xFFFFFFFFu, 0xFFFFFFFFu, 0xFFFFFFFFu, 0xFFFFFFFFu};
  STAMP(0);

  u32 t0, t1;
  {
    const int pp = tid & 255;
    const u32 colo = pp < 224 ? (u32)(8 * b * 896 + pp * 4) : OOB;
    auto kvpart = [&](auto mw) {
      constexpr bool MW = decltype(mw)::value;
      u32 ra[11], rb[8];
      const rsrc_t rk = mk(gk, S * D * 2), rv = mk(gv, S * D * 2);
      const u32 halo = (b > 0 && pp < 224) ? colo - 3 * 896 : OOB;
#pragma unroll
      for (int i = 0; i < 3; i++) ra[i] = __builtin_amdgcn_raw_buffer_load_b32(rk, halo + i * 896, 0, 0);
#pragma unroll
      for (int i = 0; i < 8; i++) ra[3 + i] = __builtin_amdgcn_raw_buffer_load_b32(rk, colo + (i & 3) * 896, (i >> 2) * 3584, 0);
      const v4u skw = __builtin_amdgcn_raw_buffer_load_b128(mk(gsk, D * 8), pp * 16, 0, 0);
#pragma unroll
      for (int i = 0; i < 8; i++) rb[i] = __builtin_amdgcn_raw_buffer_load_b32(rv, colo + (i & 3) * 896, (i >> 2) * 3584, 0);
      const v2u ep = ld64(rws, WS_EPOCH, 0);
      FENCE();
      __builtin_amdgcn_s_barrier();
      FENCE();
      USE(skw);
      STAMP(1);
      v4u tp[4], cs[28];
      if constexpr (MW) {
        const rsrc_t rt_ = mk(gssm, D * S * 2);
        const u32 o = (u32)(2 * p * 4096 + pp * 16);
#pragma unroll
        for (int c = 0; c < 2; c++) {
          tp[2 * c] = __builtin_amdgcn_raw_buffer_load_b128(rt_, pp > 0 ? o - 16 : OOB, c * 4096, 0);
          tp[2 * c + 1] = __builtin_amdgcn_raw_buffer_load_b128(rt_, o, c * 4096, 0);
        }
      }
      FENCE();
      const float s0[4] = {lo2f(skw.x), hi2f(skw.x), lo2f(skw.y), hi2f(skw.y)};
      const float s1[4] = {lo2f(skw.z), hi2f(skw.z), lo2f(skw.w), hi2f(skw.w)};
      float k0[8], k1[8];
#pragma unroll
      for (int i = 0; i < 8; i++) {
        float a0 = lo2f(ra[i + 3]) * s0[0], a1 = hi2f(ra[i + 3]) * s1[0];
#pragma unroll
        for (int j = 1; j < 4; j++) { a0 += lo2f(ra[i + 3 - j]) * s0[j]; a1 += hi2f(ra[i + 3 - j]) * s1[j]; }
        k0[i] = a0 * lo2f(rb[i]); k1[i] = a1 * hi2f(rb[i]);
      }
      *LDSP(v4u, L_RX + pp * 32) = v4u{pk(k0[3], k0[2]), pk(k0[1], k0[0]), pk(k0[7], k0[6]), pk(k0[5], k0[4])};
      *LDSP(v4u, L_RX + pp * 32 + 16) = v4u{pk(k1[3], k1[2]), pk(k1[1], k1[0]), pk(k1[7], k1[6]), pk(k1[5], k1[4])};
      t0 = __builtin_amdgcn_readfirstlane(ep.x) + 1u; t1 = __builtin_amdgcn_readfirstlane(ep.y) ^ 0x5bd1e995u;
      FENCE();
      v2u pl[4];
#pragma unroll
      for (int i = 0; i < 4; i++) pl[i] = *LDSP(v2u, L_RX + w * 2048 + lane * 8 + i * 512);
      const u32 o = WS_KVG + b * 14336 + w * 4096 + lane * 16;
#pragma unroll
      for (int i = 0; i < 4; i++)
        if (i < 2 || w < 3) st128(v4u{pl[i].x, pl[i].y, t0, t1}, rws, o + i * 1024);
      FENCE();
      STAMP(2);
      BAR();
      STAMP(3);
      if constexpr (MW) {
#pragma unroll
        for (int i = 0; i < 4; i++) OPQ(tp[i]);
#pragma unroll
        for (int c = 0; c < 2; c++) {
          const u32 a[8] = {tp[2 * c].x, tp[2 * c].y, tp[2 * c].z, tp[2 * c].w, tp[2 * c + 1].x, tp[2 * c + 1].y, tp[2 * c + 1].z, tp[2 * c + 1].w};
          u32 od[7];
#pragma unroll
          for (int j = 0; j < 7; j++) od[j] = __builtin_amdgcn_alignbit(a[j + 1], a[j], 16);
          const u32 base = L_TAP + c * 16640 + (pp + 1) * 16;
          *LDSP(v4u, base) = v4u{od[0], od[1], od[2], od[3]};
          *LDSP(v4u, base + 4160) = v4u{a[1], a[2], a[3], a[4]};
          *LDSP(v4u, base + 8320) = v4u{od[1], od[2], od[3], od[4]};
          *LDSP(v4u, base + 12480) = v4u{a[2], a[3], a[4], a[5]};
          if (pp == 255) {
            *LDSP(v2u, base + 16) = v2u{od[4], od[5]};
            *LDSP(v2u, base + 16 + 4160) = v2u{a[5], a[6]};
            *LDSP(v2u, base + 16 + 8320) = v2u{od[5], od[6]};
            *LDSP(v2u, base + 16 + 12480) = v2u{a[6], a[7]};
          }
        }
        if (tid < 8) *LDSP(v4u, L_TAP + tid * 4160) = v4u{0u, 0u, 0u, 0u};
      } else {
        BAR();
        const int j = b - 224;
        const rsrc_t rw = mk(j < 16 ? gw1 : gw3, D * F * 2);
        const u32 o = pp < 224 ? (u32)((pp / 14) * 28 * 3584 + (j & 15) * 224 + (pp % 14) * 16) : OOB;
#pragma unroll
        for (int i = 0; i < 28; i++) cs[i] = __builtin_amdgcn_raw_buffer_load_b128(rw, o, i * 3584, 0);
        float sm[8];
#pragma unroll
        for (int e = 0; e < 8; e++) sm[e] = 0.f;
#pragma unroll
        for (int i = 0; i < 28; i++)
#pragma unroll
          for (int e = 0; e < 4; e++) { sm[2 * e] += lo2f(cs[i][e]); sm[2 * e + 1] += hi2f(cs[i][e]); }
        *LDSP(v4f, pp * 32) = v4f{sm[0], sm[1], sm[2], sm[3]};
        *LDSP(v4f, pp * 32 + 16) = v4f{sm[4], sm[5], sm[6], sm[7]};
      }
    };
    if (w < 4) {
      if (mainwg) kvpart(std::true_type{}); else kvpart(std::false_type{});
    } else {
      {
        const u32 o = mainwg ? WS_HT + (u32)((8 * (w - 4) + (lane >> 4)) * 57344 + p * 256 + (lane & 15) * 16) : OOB;
        st128(poison, rws, o);
        st128(poison, rws, o + 4 * 57344);
      }
      FENCE();
      const rsrc_t rx = mk(gx, S * D * 2);
      const u32 so = lane < 56 ? (u32)((8 * b + 2 * (w - 4)) * 896 + lane * 16) : OOB;
      v4u sr[2];
      sr[0] = __builtin_amdgcn_raw_buffer_load_b128(rx, so, 0, 0);
      sr[1] = __builtin_amdgcn_raw_buffer_load_b128(rx, so + 896, 0, 0);
      u32 rb[8];
#pragma unroll
      for (int i = 0; i < 8; i++) rb[i] = __builtin_amdgcn_raw_buffer_load_b32(rx, colo + (i & 3) * 896, (i >> 2) * 3584, 0);
      const v2u ep = ld64(rws, WS_EPOCH, 0);
      FENCE();
      WAITV(11);
      __builtin_amdgcn_s_barrier();
      FENCE();
      const v4u r1 = mkv(gw1, D * F * 2), r3 = mkv(gw3, D * F * 2);
      const u32 wb = L_W + (w - 4) * 1024;
      const u32 wrow = (u32)((4 * (w - 4) + (lane >> 4)) * 3584 + fj * 256);
      const u32 vo1 = wrow + (((lane & 15) ^ (2 * (lane >> 4))) * 16), vo3 = vo1 ^ 128;
      USE(ep); USE(rb[7]);
      STAMP(1);
      if (mainwg) dmaw2<0, NE>(wb, vo1, vo3, r1, r3);
      float mean[2], rstd[2];
#pragma unroll
      for (int r = 0; r < 2; r++) {
        const float e[8] = {lo2f(sr[r].x), hi2f(sr[r].x), lo2f(sr[r].y), hi2f(sr[r].y), lo2f(sr[r].z), hi2f(sr[r].z), lo2f(sr[r].w), hi2f(sr[r].w)};
        const float s = ((e[0] + e[1]) + (e[2] + e[3])) + ((e[4] + e[5]) + (e[6] + e[7]));
        mean[r] = wsum(s) * (1.f / D);
        float q = 0.f;
#pragma unroll
        for (int i = 0; i < 8; i++) { const float d = e[i] - mean[r]; q += d * d; }
        q = wsum(lane < 56 ? q : 0.f);
        rstd[r] = __builtin_amdgcn_rsqf(q * (1.f / D) + 1e-5f);
      }
      if (lane == 0) *LDSP(v4f, L_STAT + (w - 4) * 16) = v4f{mean[0], rstd[0], mean[1], rstd[1]};
      if (tid == 256) *LDSP(u32, L_CNT) = 0u;
      t0 = __builtin_amdgcn_readfirstlane(ep.x) + 1u; t1 = __builtin_amdgcn_readfirstlane(ep.y) ^ 0x5bd1e995u;
      STAMP(2);
      BAR();
      STAMP(3);
      v4f sa[4];
#pragma unroll
      for (int i = 0; i < 4; i++) sa[i] = *LDSP(v4f, L_STAT + i * 16);
      const u32 xb = L_W + 12 * 8192 + (w - 4) * 1024;
#pragma unroll
      for (int h = 0; h < 2; h++) {
        const v4f a = sa[2 * h], c = sa[2 * h + 1];
        const u32 *x = rb + 4 * h;
        *LDSP(v4u, xb + (lane >> 4) * 4096 + (lane & 15) * 64 + h * 16) =
            v4u{plo(x[0], x[1]), plo(x[2], x[3]), pk((lo2f(x[0]) - a.x) * a.y, (lo2f(x[1]) - a.z) * a.w), pk((lo2f(x[2]) - c.x) * c.y, (lo2f(x[3]) - c.z) * c.w)};
        *LDSP(v4u, xb + (lane >> 4) * 4096 + (lane & 15) * 64 + 32 + h * 16) =
            v4u{phi(x[0], x[1]), phi(x[2], x[3]), pk((hi2f(x[0]) - a.x) * a.y, (hi2f(x[1]) - a.z) * a.w), pk((hi2f(x[2]) - c.x) * c.y, (hi2f(x[3]) - c.z) * c.w)};
      }
      FENCE();
      v4u px[4];
#pragma unroll
      for (int i = 0; i < 4; i++) px[i] = *LDSP(v4u, xb + i * 4096 + lane * 16);
      const u32 o = WS_XQ + b * 14336 + (w - 4) * 4096 + lane * 16;
#pragma unroll
      for (int i = 0; i < 4; i++)
        if (i < 2 || w < 7) st128(px[i], rws, o + i * 1024);
      FENCE();
      {
        const u32 ho = mainwg ? WS_HF + (u32)((rt * 8 * 56 + 4 * fj + (w - 4)) * 1024 + lane * 16) : OOB;
#pragma unroll
        for (int i = 0; i < 8; i++) st128(poison, rws, ho + i * 56 * 1024);
      }
      WAITV(8);
      STAMP(4);
      if (lane == 0) {
        const u32 old = __hip_atomic_fetch_add(LDSP(u32, L_CNT), 1u, __ATOMIC_RELAXED, __HIP_MEMORY_SCOPE_WORKGROUP);
        if (old == 3u) st64(t0, t1, rws, WS_FLAG + b * 8);
      }
      FENCE();
      if (mainwg) dmaw2<NE, 14>(wb, vo1, vo3, r1, r3);
      if (!mainwg) BAR();
      STAMP(5);
    }
  }
  PHASE();
  if (!mainwg) {
    BAR();
    if (w > 0) {
      if (HSLP) __builtin_amdgcn_s_sleep(HSLP);
      const v4u rw2 = mkv(gw2, F * D * 2);
      const int q = tid - 64, r = q / 7;
      const u32 o = (u32)(((r >> 3) * 224 + (ii - 28) * 8 + (r & 7)) * 896 + (q % 7) * 128);
#pragma unroll
      for (int t = 0; t < 7; t++) warm(o + t * 28672, rw2);
      WAITV(0);
    }
    if (w == 0) {
      v2f s = {0.f, 0.f};
#pragma unroll
      for (int g = 0; g < 16; g++) s += *LDSP(v2f, g * 448 + lane * 8);
      st128(v4u{f2u(s.x), f2u(s.y), t0, t1}, rws, lane < 56 ? WS_CS + (b - 224) * 896 + lane * 16 : OOB);
    }
    STAMP(23);
    STOUT();
    return;
  }

  if (w < 4) {
    STAMP(4);
    if (DLY1) __builtin_amdgcn_s_sleep(DLY1);
    v4u g[4];
    const u32 o = WS_KVG + (tid >> 2) * 14336 + p * 64 + (tid & 3) * 16;
#pragma unroll
    for (int i = 0; i < 4; i++) g[i] = ld128(rws, o, i * 64 * 14336);
#pragma nounroll
    for (u32 spin = 0; spin < SPIN_MAX; spin++) {
      bool bad[4], any = false;
#pragma unroll
      for (int i = 0; i < 4; i++) { bad[i] = (g[i].z != t0) | (g[i].w != t1); any |= bad[i]; }
      if (!__any(any)) break;
      __builtin_amdgcn_s_sleep(PSLP);
      FENCE();
#pragma unroll
      for (int i = 0; i < 4; i++) if (bad[i]) g[i] = ld128(rws, o, i * 64 * 14336);
    }
    STAMP(5);
    const u32 la = L_RX + ((tid >> 1) & 1) * 4608 + (508 - 8 * (tid >> 2) - 4 * (tid & 1)) * 2;
#pragma unroll
    for (int i = 0; i < 4; i++) *LDSP(v2u, la + (3 - i) * 1024) = v2u{g[i].x, g[i].y};
    if (tid < 64) *LDSP(v4u, L_RX + (tid >> 5) * 4608 + 4096 + (tid & 31) * 16) = v4u{0u, 0u, 0u, 0u};
  }
  const int ch = w >> 2, wq = w & 3;
  const float dval = lo2f(gsd[2 * p + ch]);
  const u32 fo1 = WS_FLAG + (64 * wq + lane) * 8;
  BAR();
  STAMP(6);
  v4u w13[28];
  const u32 wra = L_W + (((lane >> 4) & 1) * 4 + 2 * (lane >> 5)) * 1024 + ((lane & 15) >> 2) * 256 +
                  (((2 * w + (lane & 1)) ^ (2 * ((lane & 15) >> 2)) ^ (8 * ((lane >> 4) & 1))) * 16) + ((lane >> 1) & 1) * 8;
  {
    const v2u f1 = ld64(rws, fo1, 0);
    FENCE();
    const int la_ = lane & 15, kg = lane >> 4;
    v4f acc[8];
#pragma unroll
    for (int n = 0; n < 8; n++) acc[n] = v4f{0.f, 0.f, 0.f, 0.f};
    u32 aa = L_TAP + ch * 16640 + (la_ & 3) * 4160 + ((la_ >> 2) + 2 * kg + 16 * wq) * 8, ab = aa + 8;
    asm volatile("" : "+v"(ab));
    const u32 ba = L_RX + ch * 4608 + (30 + 8 * wq - 2 * la_ + kg) * 16;
    auto lda = [&](int o) { const v2u x = *LDSP(v2u, aa + o), y = *LDSP(v2u, ab + o); return v4u{x.x, x.y, y.x, y.y}; };
    v4u A[8], B[8], A2[8], B2[8];
#pragma unroll
    for (int m = 0; m < 8; m++) { A[m] = lda(m * 512); B[m] = *LDSP(v4u, ba + (7 - m) * 512); SCHED(); }
#pragma unroll
    for (int g = 0; g < 8; g++) {
      A2[g] = lda(64 + g * 512); B2[g] = *LDSP(v4u, ba + 64 + (7 - g) * 512);
#pragma unroll
      for (int m = 0; m <= g; m++) {
        if (m + g < 8) acc[m + g] = MFMA16(A[m], B[g], acc[m + g]);
        if (m < g && m + g < 8) acc[m + g] = MFMA16(A[g], B[m], acc[m + g]);
      }
      SCHED();
    }
    const int t = 512 * wq + 16 * la_ + 4 * kg;
    const v2u kv0 = *LDSP(v2u, L_RX + ch * 4608 + (2044 - t) * 2), kv1 = *LDSP(v2u, L_RX + ch * 4608 + (2044 - 256 - t) * 2);
    waitflag(f1, rws, fo1, t0, t1);
    WAITV(0);
    STAMP(7);
    BAR();
    STAMP(8);
    const u32 xqo = WS_XQ + (64 * wq + 2 * la_ + (kg >> 1)) * 14336 + p * 64 + ch * 32 + (kg & 1) * 16;
    const v4u xq0 = ld128(rws, xqo, 0), xq1 = ld128(rws, xqo, 32 * 14336);
    FENCE();
#pragma unroll
    for (int d = 7; d >= 0; d--) {
      if (d > 0) { w13[14 - 2 * d] = trd2<1024>(wra + (14 - 2 * d) * 8192); w13[15 - 2 * d] = trd2<1024>(wra + (15 - 2 * d) * 8192); }
#pragma unroll
      for (int m = 0; m <= d; m++) acc[d] = MFMA16(A2[m], B2[d - m], acc[d]);
      SCHED();
    }
#pragma unroll
    for (int n = 7; n >= 0; n--)
      if ((n >> 1) != wq) *LDSP(v4f, L_RED + (w * 6 + (n < 2 * wq ? n : n - 2)) * 1024 + lane * 16) = acc[n];
    v4f y0, y1;
#pragma unroll
    for (int n = 0; n < 4; n++) if (n == wq) { y0 = acc[2 * n]; y1 = acc[2 * n + 1]; }
    BAR();
    STAMP(9);
#pragma unroll
    for (int sq = 1; sq < 4; sq++) {
      const int sw = (wq + sq) & 3;
      const u32 a = L_RED + ((ch * 4 + sw) * 6 + (wq < sw ? 2 * wq : 2 * wq - 2)) * 1024 + lane * 16;
      y0 += *LDSP(v4f, a);
      y1 += *LDSP(v4f, a + 1024);
    }
    const u32 h0x = pk(lo2f(xq0.x) + lo2f(xq0.z) * (y0.x + hi2f(kv0.y) * dval), hi2f(xq0.x) + hi2f(xq0.z) * (y0.y + lo2f(kv0.y) * dval));
    const u32 h0y = pk(lo2f(xq0.y) + lo2f(xq0.w) * (y0.z + hi2f(kv0.x) * dval), hi2f(xq0.y) + hi2f(xq0.w) * (y0.w + lo2f(kv0.x) * dval));
    const u32 h1x = pk(lo2f(xq1.x) + lo2f(xq1.z) * (y1.x + hi2f(kv1.y) * dval), hi2f(xq1.x) + hi2f(xq1.z) * (y1.y + lo2f(kv1.y) * dval));
    const u32 h1y = pk(lo2f(xq1.y) + lo2f(xq1.w) * (y1.z + hi2f(kv1.x) * dval), hi2f(xq1.y) + hi2f(xq1.w) * (y1.w + lo2f(kv1.x) * dval));
    const int s1 = (wq + 1) & 3;
    const u32 tb = L_RED + ((ch * 4 + s1) * 6 + (wq < s1 ? 2 * wq : 2 * wq - 2)) * 1024;
    *LDSP(v2u, tb + la_ * 32 + kg * 8) = v2u{canon(h0x), canon(h0y)};
    *LDSP(v2u, tb + 512 + la_ * 32 + kg * 8) = v2u{canon(h1x), canon(h1y)};
    FENCE();
    st128(*LDSP(v4u, tb + lane * 16), rws, WS_HT + (u32)((8 * wq + (lane >> 3)) * 57344 + (2 * p + ch) * 128 + (lane & 7) * 16));
  }
  FENCE();
  STAMP(10);
  PHASE();

  {
    const v4u rw = mkv(w < 4 ? gw1 : gw3, D * F * 2);
    const u32 vo = (u32)((4 * wq + (lane >> 4)) * 3584 + fj * 256) + (((lane & 15) ^ (2 * (lane >> 4)) ^ (8 * ch)) * 16);
    dmaw<14, 28, 0>(L_W + w * 1024, vo, rw);
  }
  int hh = lane >> 5;
  u32 hfo = WS_HF + (u32)(((rt * 2 + ((lane >> 4) & 1)) * 4 * 56 + 4 * fj + (w >> 1)) * 1024 + ((2 * (w & 1) + hh) * 16 + (lane & 15)) * 16);
  OPQ(hfo);
  FENCE();
  if (DLY2) __builtin_amdgcn_s_sleep(DLY2);
  v4u hc[14];
  const u32 hto = WS_HT + rt * 114688 - 8192 + (tid >> 2) * 128 + (tid & 3) * 16;
  const u32 so3 = w < 4 ? 57344u : 64u;
  auto hso = [&](int i) -> u32 { const int j = i % 7; return (i / 7) * 57344u + (j < 3 ? 8192u + j * 16384u : j == 3 ? so3 : 64u + 16384u * (j - 3)); };
#pragma unroll
  for (int i = 0; i < 4; i++) hc[i] = ld128(rws, hto, hso(i));
  FENCE();
  settle<4>(hc, rws, hto, hso);
  WAITV(0);
#pragma unroll
  for (int i = 4; i < 14; i++) hc[i] = ld128(rws, hto, hso(i));
  const u32 cso = w >= 6 ? WS_CS + (u32)((w - 6) * 14336 + fj * 1024 + lane * 16) : OOB;
  v4u csl = ld128(rws, cso, 0);
  FENCE();
  STAMP(11);
  BAR();
#pragma unroll
  for (int s = 14; s < 28; s++) w13[s] = trd2<1024>(wra + (s - 14) * 8192);
#pragma unroll
  for (int i = 0; i < 3; i++) *LDSP(v4u, L_Q0 + tid * 16 + i * 8192) = hc[i];
  if (w < 4) *LDSP(v4u, L_Q0 + tid * 16 + 24576) = hc[3];
  BAR();
  STAMP(12);
  const v4u ones = {0x3F803F80u, 0x3F803F80u, 0x3F803F80u, 0x3F803F80u};
  const u32 trb = (8 * hh + ((lane & 15) >> 2)) * 64 + ((lane >> 4) & 1) * 32 + (lane & 3) * 8;
  const u32 lw = L_Q1 + 4096 + tid * 16;
  auto stats = [&](auto ns, int rg, int s0, int slot) {
    constexpr int NS = decltype(ns)::value;
    const u32 sb = (rg < 2 ? L_Q0 : L_Q1 + ((rg >> 1) - 1) * 28672) + s0 * 2048 + (8 * (lane >> 4) + ((lane & 15) >> 2)) * 64 + (rg & 1) * 32 + (lane & 3) * 8;
    v4f g = {0.f, 0.f, 0.f, 0.f}, sm = {0.f, 0.f, 0.f, 0.f};
    v4u fr[NS];
#pragma unroll
    for (int s = 0; s < NS; s++) fr[s] = trd2<256>(sb + s * 2048);
#pragma unroll
    for (int s = 0; s < NS; s++) { g = MFMA16(fr[s], fr[s], g); sm = MFMA16(ones, fr[s], sm); }
    const int i = lane & 3;
    const float sq = i == 0 ? g.x : i == 1 ? g.y : i == 2 ? g.z : g.w;
    if (((lane & 15) >> 2) == (lane >> 4)) {
      const u32 a = L_ST + (16 * rg + (lane & 15)) * 16;
      if (NS == 14) *LDSP(v4f, a) = v4f{sm.x, sq, 0.f, 0.f}; else *LDSP(v2f, a + slot * 8) = v2f{sm.x, sq};
    }
  };
  float nm, ny, cv;
  auto getst = [&](int nt) {
    const v4f s = *LDSP(v4f, L_ST + (32 * nt + (lane & 31)) * 16);
    nm = opq(s.x + s.z) * (-1.f / D);
    cv = fmaxf(__builtin_fmaf(-nm, nm, opq(s.y + s.w) * (1.f / D)), 0.f) + 1e-5f;
    ny = -1.4426950408889634f * __builtin_amdgcn_rsqf(cv);
  };
  auto epi = [&](float a, float u, float c1, float c3) -> float {
    const float g = opq(__builtin_fmaf(nm, c1, a)), uu = opq(__builtin_fmaf(nm, c3, u));
    const float e = __builtin_amdgcn_exp2f(opq(g * ny));
    return opq(opq(g * uu) * __builtin_amdgcn_rcpf(opq(__builtin_fmaf(e, cv, cv))));
  };
  if (w < 4) stats(std::integral_constant<int, 7>{}, w >> 1, 7 * (w & 1), w & 1);
  SCHED();
  const v4u rw2 = mkv(gw2, F * D * 2);
  u32 w2o = (u32)((224 * w + (lane >> 3)) * 896 + dt * 128 + ((lane & 7) ^ ((lane >> 3) & 2)) * 16);
  u32 w2a = (u32)((lane >> 4) * 1024 + ((lane & 15) >> 2) * 128 + (((lane >> 3) & 1) | ((lane >> 3) & 2)) * 32 + (lane & 3) * 8);
  OPQ(w2o); OPQ(w2a);
  const u32 slA = w < 7 ? 86016u + w * 4096u : 147456u, slB = w < 7 ? L_Q0 + w * 4096u : 151552u, slC = w < 7 ? L_Q1 + w * 4096u : 155648u;
  v4u wa[7][4];
  auto rdw = [&](int t, u32 sl) {
    const u32 a = w2a + sl;
#pragma unroll
    for (int j = 0; j < 4; j++) wa[t][j] = trd2<512>(a ^ (32u * j));
  };
  auto usew = [&](int t) { asm volatile("" ::"v"(wa[t][0]), "v"(wa[t][1]), "v"(wa[t][2]), "v"(wa[t][3])); };
  auto rdres = [&]() {
    return trd2<256>(((w >> 1) == 0 ? L_Q0 : L_Q1 + ((w >> 1) - 1) * 28672) + (64 * dt + 16 * (lane >> 4) + 8 * ((lane >> 1) & 1) + ((lane & 15) >> 2)) * 64 + 32 * q2 + 16 * (w & 1) + 8 * (lane & 1));
  };
  const u32 h2o = WS_HF + (u32)(((rt * 2 + q2) * 4 * 56 + 7 * w) * 1024 + lane * 16);
  const u32 csa = L_CSV + (16 * w + 8 * hh) * 4;
  auto lin = [](int i) -> u32 { return i * 1024u; };
  v4u hf[4][7], res;
  v16f ac[4];
#define PDX 4
  constexpr int PD = PDX;
#pragma unroll
  for (int nt = 0; nt < 4; nt++) {
    u32 tb_ = trb + (nt == 0 ? L_Q0 : L_Q1 + (nt - 1) * 28672);
    OPQ(tb_);
    v4u bfr[28];
#pragma unroll
    for (int s = 0; s < PD; s++) bfr[s] = trd2<256>(tb_ + s * 1024);
    float hv[8];
    v4f c1, c3;
    if (nt > 0) getst(nt - 1);
#pragma unroll
    for (int r = 0; r < 16; r++) ac[nt][r] = 0.f;
    SCHED();
#pragma unroll
    for (int s = 0; s < 28; s++) {
      if (s + PD < 28) bfr[s + PD] = trd2<256>(tb_ + (s + PD) * 1024);
      if (nt > 0 && (s == 0 || s == 9)) { c1 = *LDSP(v4f, csa + (s ? 16 : 0)); c3 = *LDSP(v4f, csa + 512 + (s ? 16 : 0)); }
      ac[nt] = MFMA32(w13[s], bfr[s], ac[nt]);
      if (nt == 0) {
        if (s == 8) settle<3>(hc + 4, rws, hto, [&](int i) { return hso(i + 4); });
        if (s == 7 && w >= 4) *LDSP(v4u, L_Q1 + (tid & 255) * 16) = hc[3];
        if (s >= 8 && s < 11) *LDSP(v4u, lw + (s - 8) * 8192) = hc[s - 4];
        if (s == 15) settle<4>(hc + 7, rws, hto, [&](int i) { return hso(i + 7); });
        if (s >= 15 && s < 19) *LDSP(v4u, lw + (s - 12) * 8192) = hc[s - 8];
        if (s == 22) settle<3>(hc + 11, rws, hto, [&](int i) { return hso(i + 11); });
        if (s >= 22 && s < 25) *LDSP(v4u, lw + (s - 15) * 8192) = hc[s - 11];
      } else {
        if (s >= 2 && s < 18 && (s & 1) == 0) { const int r = (s - 2) >> 1; hv[r] = epi(ac[nt - 1][r], ac[nt - 1][r + 8], c1[r & 3], c3[r & 3]); }
        if (s == 18) st128(v4u{canon(pk(hv[0], hv[1])), canon(pk(hv[2], hv[3])), canon(pk(hv[4], hv[5])), canon(pk(hv[6], hv[7]))}, rws, hfo);
        if (s == 19) { hfo += 56 * 1024; OPQ(hfo); }
      }
      if (nt == 0 && s == 25) {
        if (w >= 6) {
          if (UNLIKELY(__any((csl.z != t0) | (csl.w != t1)))) {
#pragma nounroll
            for (u32 spin = 0; spin < SPIN_MAX; spin++) {
              FENCE();
              csl = ld128(rws, cso, 0);
              if (!__any((csl.z != t0) | (csl.w != t1))) break;
            }
            DRAINV();
          }
          *LDSP(v2u, L_CSV + (tid - 384) * 8) = v2u{csl.x, csl.y};
        }
        dmak<0>(slA, w2o, rw2);
      }
      if (nt == 1) {
        if (s == 0) dmak<1>(slB, w2o, rw2);
        if (s == 6) { WAITV(4); rdw(0, slA); }
        if (s == 9) { usew(0); dmak<2>(slA, w2o, rw2); }
        if (s == 16) { WAITV(4); rdw(1, slB); }
        if (s == 19) { usew(1); dmak<3>(slB, w2o, rw2); }
      }
      if (nt == 2) {
        if (s == 0) dmak<4>(slC, w2o, rw2);
        if (s == 4) { WAITV(9); rdw(2, slA); }
        if (s == 7) { usew(2); dmak<5>(slA, w2o, rw2); }
      }
      if (nt == 3) {
        if (s == 4) { WAITV(1); rdw(5, slA); }
        if (s == 7) { usew(5); dmak<6>(slA, w2o, rw2); }
        if (s == 8) rdw(3, slB);
        if (s == 12) rdw(4, slC);
        if (s >= 13 && s < 20) hf[0][s - 13] = ld128(rws, h2o, (s - 13) * 1024);
        if (s >= 20 && s < 27) hf[1][s - 20] = ld128(rws, h2o, (56 + s - 20) * 1024);
      }
      SCHED();
    }
    STAMP(13 + nt);
    if (nt == 0) {
      if (w < 2) res = rdres();
      BAR();
      if (w >= 2) res = rdres();
      if (w < 4) stats(std::integral_constant<int, 7>{}, 2 + (w >> 1), 7 * (w & 1), w & 1);
      else stats(std::integral_constant<int, 14>{}, w, 0, 0);
    }
    if (nt == 1) BAR();
    SCHED();
  }
  {
    getst(3);
    float hv[8];
#pragma unroll
    for (int h = 0; h < 2; h++) {
      const v4f c1 = *LDSP(v4f, csa + 16 * h), c3 = *LDSP(v4f, csa + 512 + 16 * h);
#pragma unroll
      for (int r = 0; r < 4; r++) hv[4 * h + r] = epi(ac[3][4 * h + r], ac[3][4 * h + r + 8], c1[r], c3[r]);
    }
    st128(v4u{canon(pk(hv[0], hv[1])), canon(pk(hv[2], hv[3])), canon(pk(hv[4], hv[5])), canon(pk(hv[6], hv[7]))}, rws, hfo);
  }
  FENCE();
#pragma unroll
  for (int s = 0; s < 7; s++) hf[2][s] = ld128(rws, h2o, (2 * 56 + s) * 1024);
  FENCE();
  WAITV(23);
  rdw(6, slA);
  STAMP(17);
  PHASE();
  const u32 pw = (u32)(w * 4096 + lane * 16);
  auto finalize = [&]() {
    const int n = 8 * (w & 1) + (lane & 7);
    const u32 a = (u32)((w >> 1) * 32768 + (lane >> 3) * 512 + (n ^ ((lane >> 1) & 8)) * 16);
    v4f e = *LDSP(v4f, a), o = *LDSP(v4f, a + 256);
#pragma unroll
    for (int u = 1; u < 8; u++) { e += *LDSP(v4f, a + u * 4096); o += *LDSP(v4f, a + u * 4096 + 256); }
    const v4u ov = {pk(lo2f(res.x) + e.x, hi2f(res.x) + e.y), pk(lo2f(res.y) + e.z, hi2f(res.y) + e.w),
                    pk(lo2f(res.z) + o.x, hi2f(res.z) + o.y), pk(lo2f(res.w) + o.z, hi2f(res.w) + o.w)};
    st128(ov, mk(gout, S * D * 2), (u32)(((128 * rt + 32 * (w >> 1) + 16 * q2 + n) * D + 64 * dt + 8 * (lane >> 3)) * 2));
  };
#pragma unroll
  for (int g = 0; g < 4; g++) {
    if (g == 3) {
      BAR();
      STAMP(22);
      if (w < 6) finalize();
      FENCE();
    }
    settle<7>(hf[g], rws, h2o + g * 56 * 1024, lin);
    STAMP(18 + g);
    v4f c[4];
#pragma unroll
    for (int i = 0; i < 4; i++) c[i] = v4f{0.f, 0.f, 0.f, 0.f};
#pragma unroll
    for (int s = 0; s < 7; s++)
#pragma unroll
      for (int i = 0; i < 4; i++) c[i] = MFMA16(wa[s][i], hf[g][s], c[i]);
    SCHED();
    if (g == 0) BAR();
    if (g == 1) {
#pragma unroll
      for (int s = 0; s < 7; s++) hf[3][s] = ld128(rws, h2o, (3 * 56 + s) * 1024);
      FENCE();
    }
#pragma unroll
    for (int i = 0; i < 4; i++) *LDSP(v4f, g * 32768 + (pw ^ (128u * (i & 1))) + i * 1024) = c[i];
    SCHED();
  }
  BAR();
  if (w >= 6) finalize();
  if (b == 0 && tid == 0) st64(t0, t1 ^ 0x5bd1e995u, rws, WS_EPOCH);
  STAMP(23);
  STOUT();
}

extern "C" void kernel_launch(void* const* d_in, const int* in_sizes, int n_in, void* d_out, int out_size, void* d_ws, size_t ws_size,
                              hipStream_t stream) {
  (void)hipFuncSetAttribute((const void*)fused_kernel, hipFuncAttributeMaxDynamicSharedMemorySize, L_TOTAL);
  fused_kernel<<<256, 512, L_TOTAL, stream>>>((const u16*)d_in[0], (const u16*)d_in[1], (const u16*)d_in[2], (const u16*)d_in[3],
                                              (const u16*)d_in[4], (const u16*)d_in[5], (const u16*)d_in[7], (const u16*)d_in[8],
                                              (const u16*)d_in[9], (u16*)d_out, (unsigned char*)d_ws);
}
```

```cpp
#include <hip/hip_runtime.h>
#include <hip/hip_bf16.h>
#include <type_traits>

typedef unsigned u32;
typedef unsigned short u16;
typedef unsigned long long u64;
typedef u32 v4u __attribute__((ext_vector_type(4)));
typedef u32 v2u __attribute__((ext_vector_type(2)));
typedef float v4f __attribute__((ext_vector_type(4)));
typedef float v2f __attribute__((ext_vector_type(2)));
typedef float v16f __attribute__((ext_vector_type(16)));
typedef __bf16 bf8 __attribute__((ext_vector_type(8)));
typedef __bf16 bf2 __attribute__((ext_vector_type(2)));
typedef short s4 __attribute__((ext_vector_type(4)));
typedef __amdgpu_buffer_rsrc_t rsrc_t;
#define LDSP(T, off) ((__attribute__((address_space(3))) T*)(size_t)(off))

constexpr int S = 2048, D = 448, F = 1792;
constexpr u32 WS_EPOCH = 0;
constexpr u32 WS_FLAG = 4096;
constexpr u32 WS_CS = 16384;
constexpr u32 WS_KVG = 49152;
constexpr u32 WS_XQ = WS_KVG + 256u * 224 * 64;
constexpr u32 WS_HT = WS_XQ + 256u * 224 * 64;
constexpr u32 WS_HF = WS_HT + 32u * 448 * 128;
constexpr u32 WS_END = WS_HF + 128u * 56 * 1024;
constexpr u32 WS_DBG = WS_END;
constexpr u32 L_W = 0;
constexpr u32 L_TAP = 114688;
constexpr u32 L_RX = 147968;
constexpr u32 L_DUM = 157184;
constexpr u32 L_STAT = 162816, L_CNT = L_STAT + 64;
constexpr u32 L_RED = 114688;
constexpr u32 L_Q0 = 114688, L_Q1 = 0;
constexpr u32 L_ST = 143360;
constexpr u32 L_CSV = 145408;
constexpr u32 L_TOTAL = 160 * 1024;
constexpr u32 OOB = 0x80000000u;
constexpr int SC1 = 16;
constexpr u32 SPIN_MAX = 1u << 16;

#define FENCE() asm volatile("" ::: "memory")
#define WAITV(n) asm volatile("s_waitcnt vmcnt(" #n ")" ::: "memory")
#define DRAINV() __builtin_amdgcn_s_waitcnt(0x0F70)
#define BAR() do { asm volatile("s_waitcnt lgkmcnt(0)" ::: "memory"); __builtin_amdgcn_s_barrier(); FENCE(); } while (0)
#define SCHED() __builtin_amdgcn_sched_barrier(0)
#define PHASE() do { SCHED(); asm volatile("" : "+v"(tid)); lane = tid & 63; SCHED(); } while (0)
#define UNLIKELY(x) __builtin_expect(!!(x), 0)
#define USE(x) asm volatile("" ::"v"(x))
#define OPQ(x) asm volatile("" : "+v"(x))

__device__ __forceinline__ rsrc_t mk(const void* p, u32 bytes) { return __builtin_amdgcn_make_buffer_rsrc((void*)p, 0, bytes, 0x00020000); }
__device__ __forceinline__ v4u mkv(const void* p, u32 bytes) { const u64 a = (u64)p; return v4u{(u32)a, (u32)(a >> 32) & 0xffffu, bytes, 0x00020000u}; }
__device__ __forceinline__ float lo2f(u32 v) { return __builtin_bit_cast(float, v << 16); }
__device__ __forceinline__ float hi2f(u32 v) { return __builtin_bit_cast(float, v & 0xffff0000u); }
__device__ __forceinline__ u32 pk(float a, float b) { return __builtin_bit_cast(u32, __builtin_convertvector(v2f{a, b}, bf2)); }
__device__ __forceinline__ u32 plo(u32 a, u32 b) { return __builtin_amdgcn_perm(b, a, 0x05040100u); }
__device__ __forceinline__ u32 phi(u32 a, u32 b) { return __builtin_amdgcn_perm(b, a, 0x07060302u); }
__device__ __forceinline__ u32 canon(u32 v) { return min(v, 0xFFFFFFFEu); }
__device__ __forceinline__ u32 max4(v4u t) { return max(max(t.x, t.y), max(t.z, t.w)); }
__device__ __forceinline__ u32 max5(u32 m, v4u t) { return max(max(max(m, t.x), t.y), max(t.z, t.w)); }
__device__ __forceinline__ bf8 asbf(v4u v) { return __builtin_bit_cast(bf8, v); }
__device__ __forceinline__ v2u trd(u32 a) { return __builtin_bit_cast(v2u, __builtin_amdgcn_ds_read_tr16_b64_v4i16(LDSP(s4, a))); }
template <int O> __device__ __forceinline__ v4u trd2(u32 a) { const v2u x = trd(a), y = trd(a + O); return v4u{x.x, x.y, y.x, y.y}; }
__device__ __forceinline__ void st128(v4u v, rsrc_t r, u32 off) { __builtin_amdgcn_raw_buffer_store_b128(v, r, off, 0, SC1); }
__device__ __forceinline__ void st64(u32 a, u32 b_, rsrc_t r, u32 off) { __builtin_amdgcn_raw_buffer_store_b64(v2u{a, b_}, r, off, 0, SC1); }
__device__ __forceinline__ v4u ld128(rsrc_t r, u32 off, u32 so) { return __builtin_amdgcn_raw_buffer_load_b128(r, off, so, SC1); }
__device__ __forceinline__ v2u ld64(rsrc_t r, u32 off, u32 so) { return __builtin_amdgcn_raw_buffer_load_b64(r, off, so, SC1); }
__device__ __forceinline__ u32 f2u(float v) { return __builtin_bit_cast(u32, v); }
__device__ __forceinline__ float u2f(u32 v) { return __builtin_bit_cast(float, v); }
__device__ __forceinline__ float opq(float v) { asm("" : "+v"(v)); return v; }
template <int CTRL> __device__ __forceinline__ float dppadd(float v) { return v + u2f(__builtin_amdgcn_update_dpp(0, f2u(v), CTRL, 0xF, 0xF, true)); }
__device__ __forceinline__ float swap16add(float a, float b) { const auto r = __builtin_amdgcn_permlane16_swap(f2u(a), f2u(b), false, false); return u2f(r[0]) + u2f(r[1]); }
__device__ __forceinline__ float swap32add(float a, float b) { const auto r = __builtin_amdgcn_permlane32_swap(f2u(a), f2u(b), false, false); return u2f(r[0]) + u2f(r[1]); }
__device__ __forceinline__ float wsum(float v) {
  v = dppadd<0xB1>(v); v = dppadd<0x4E>(v); v = dppadd<0x141>(v); v = dppadd<0x140>(v);
  v = swap16add(v, v);
  return swap32add(v, v);
}
template <u32 LI, u32 SO> __device__ __forceinline__ void dma16(u32 lb, u32 vo, v4u r) {
  u32 t;
  asm volatile("s_add_u32 m0, %1, %4\n\ts_mov_b32 %0, %5\n\tbuffer_load_dwordx4 %2, %3, %0 offen lds"
               : "=&s"(t) : "s"(lb), "v"(vo), "s"(r), "n"(LI), "n"(SO) : "memory", "scc");
}
__device__ __forceinline__ void warm(u32 vo, v4u r) {
  asm volatile("s_mov_b32 m0, %2\n\ts_nop 0\n\tbuffer_load_dword %0, %1, 0 offen lds" ::"v"(vo), "s"(r), "n"(L_DUM) : "memory");
}
template <int S0, int S1, int MS> __device__ __forceinline__ void dmaw(u32 lb, u32 vo, v4u r) {
  if constexpr (S0 < S1) { dma16<(S0 % 14) * 8192u + MS * 4096u, S0 * 57344u>(lb, vo, r); dmaw<S0 + 1, S1, MS>(lb, vo, r); }
}
template <int S0, int S1> __device__ __forceinline__ void dmaw2(u32 lb, u32 vo1, u32 vo3, v4u r1, v4u r3) {
  if constexpr (S0 < S1) { dma16<S0 * 8192u, S0 * 57344u>(lb, vo1, r1); dma16<S0 * 8192u + 4096u, S0 * 57344u>(lb, vo3, r3); dmaw2<S0 + 1, S1>(lb, vo1, vo3, r1, r3); }
}
template <int T> __device__ __forceinline__ void dmak(u32 lb, u32 vo, v4u r) {
  const u32 v1 = vo ^ 64u;
  dma16<0u, T * 28672u>(lb, vo, r); dma16<1024u, T * 28672u + 7168u>(lb, v1, r);
  dma16<2048u, T * 28672u + 14336u>(lb, vo, r); dma16<3072u, T * 28672u + 21504u>(lb, v1, r);
}
#define MFMA32(a, b, c) __builtin_amdgcn_mfma_f32_32x32x16_bf16(asbf(a), asbf(b), c, 0, 0, 0)
#define MFMA16(a, b, c) __builtin_amdgcn_mfma_f32_16x16x32_bf16(asbf(a), asbf(b), c, 0, 0, 0)
#define STAMP(i)
#define STOUT()
#define DLY1 0
#define DLY2 0
#define PSLP 4
#define HSLP 0
#define NE 2

template <int N, class SOF> __device__ __forceinline__ void settle(v4u* t, rsrc_t r, u32 off, SOF so) {
  u32 m = 0;
#pragma unroll
  for (int i = 0; i < N; i++) m = max5(m, t[i]);
  if (UNLIKELY(__any(m == 0xFFFFFFFFu))) {
#pragma nounroll
    for (u32 spin = 0; spin < SPIN_MAX; spin++) {
      bool any = false;
      FENCE();
#pragma unroll
      for (int i = 0; i < N; i++)
        if (__any(max4(t[i]) == 0xFFFFFFFFu)) { t[i] = ld128(r, off, so(i)); any = true; }
      if (!any) break;
    }
    DRAINV();
  }
}
__device__ __forceinline__ void waitflag(v2u f, rsrc_t r, u32 off, u32 t0, u32 t1) {
  if (UNLIKELY(__any((f.x != t0) | (f.y != t1)))) {
#pragma nounroll
    for (u32 spin = 0; spin < SPIN_MAX; spin++) {
      FENCE();
      f = ld64(r, off, 0);
      if (!__any((f.x != t0) | (f.y != t1))) break;
    }
    DRAINV();
  }
  FENCE();
}

__global__ void __launch_bounds__(512, 2)
fused_kernel(const u16* __restrict__ gx, const u16* __restrict__ gk, const u16* __restrict__ gv, const u16* __restrict__ gsk,
             const u16* __restrict__ gssm, const u16* __restrict__ gsd, const u16* __restrict__ gw1, const u16* __restrict__ gw3,
             const u16* __restrict__ gw2, u16* __restrict__ gout, unsigned char* __restrict__ ws) {
  asm volatile("" ::"s"(gx), "s"(gk), "s"(gv), "s"(gsk), "s"(gssm), "s"(gsd), "s"(gw1), "s"(gw3), "s"(gw2), "s"(gout), "s"(ws));
  int tid = threadIdx.x, lane = tid & 63;
  const int w = __builtin_amdgcn_readfirstlane(tid >> 6), b = blockIdx.x;
  const int xc = b & 7, ii = b >> 3;
  const bool mainwg = ii < 28;
  const int p = xc * 28 + ii;
  const int dt = ii % 7, q2 = xc & 1;
  const int rt = 4 * (xc >> 1) + ii / 7, fj = 7 * q2 + dt;
  const rsrc_t rws = mk(ws, WS_END);
  const v4u poison = {0xFFFFFFFFu, 0xFFFFFFFFu, 0xFFFFFFFFu, 0xFFFFFFFFu};
  STAMP(0);

  u32 t0, t1;
  {
    const int pp = tid & 255;
    const u32 colo = pp < 224 ? (u32)(8 * b * 896 + pp * 4) : OOB;
    auto kvpart = [&](auto mw) {
      constexpr bool MW = decltype(mw)::value;
      u32 ra[11], rb[8];
      const rsrc_t rk = mk(gk, S * D * 2), rv = mk(gv, S * D * 2);
      const u32 halo = (b > 0 && pp < 224) ? colo - 3 * 896 : OOB;
#pragma unroll
      for (int i = 0; i < 3; i++) ra[i] = __builtin_amdgcn_raw_buffer_load_b32(rk, halo + i * 896, 0, 0);
#pragma unroll
      for (int i = 0; i < 8; i++) ra[3 + i] = __builtin_amdgcn_raw_buffer_load_b32(rk, colo + (i & 3) * 896, (i >> 2) * 3584, 0);
      const v4u skw = __builtin_amdgcn_raw_buffer_load_b128(mk(gsk, D * 8), pp * 16, 0, 0);
#pragma unroll
      for (int i = 0; i < 8; i++) rb[i] = __builtin_amdgcn_raw_buffer_load_b32(rv, colo + (i & 3) * 896, (i >> 2) * 3584, 0);
      const v2u ep = ld64(rws, WS_EPOCH, 0);
      FENCE();
      __builtin_amdgcn_s_barrier();
      FENCE();
      USE(skw);
      STAMP(1);
      v4u tp[4], cs[28];
      if constexpr (MW) {
        const rsrc_t rt_ = mk(gssm, D * S * 2);
        const u32 o = (u32)(2 * p * 4096 + pp * 16);
#pragma unroll
        for (int c = 0; c < 2; c++) {
          tp[2 * c] = __builtin_amdgcn_raw_buffer_load_b128(rt_, pp > 0 ? o - 16 : OOB, c * 4096, 0);
          tp[2 * c + 1] = __builtin_amdgcn_raw_buffer_load_b128(rt_, o, c * 4096, 0);
        }
      }
      FENCE();
      const float s0[4] = {lo2f(skw.x), hi2f(skw.x), lo2f(skw.y), hi2f(skw.y)};
      const float s1[4] = {lo2f(skw.z), hi2f(skw.z), lo2f(skw.w), hi2f(skw.w)};
      float k0[8], k1[8];
#pragma unroll
      for (int i = 0; i < 8; i++) {
        float a0 = lo2f(ra[i + 3]) * s0[0], a1 = hi2f(ra[i + 3]) * s1[0];
#pragma unroll
        for (int j = 1; j < 4; j++) { a0 += lo2f(ra[i + 3 - j]) * s0[j]; a1 += hi2f(ra[i + 3 - j]) * s1[j]; }
        k0[i] = a0 * lo2f(rb[i]); k1[i] = a1 * hi2f(rb[i]);
      }
      *LDSP(v4u, L_RX + pp * 32) = v4u{pk(k0[3], k0[2]), pk(k0[1], k0[0]), pk(k0[7], k0[6]), pk(k0[5], k0[4])};
      *LDSP(v4u, L_RX + pp * 32 + 16) = v4u{pk(k1[3], k1[2]), pk(k1[1], k1[0]), pk(k1[7], k1[6]), pk(k1[5], k1[4])};
      t0 = __builtin_amdgcn_readfirstlane(ep.x) + 1u; t1 = __builtin_amdgcn_readfirstlane(ep.y) ^ 0x5bd1e995u;
      FENCE();
      v2u pl[4];
#pragma unroll
      for (int i = 0; i < 4; i++) pl[i] = *LDSP(v2u, L_RX + w * 2048 + lane * 8 + i * 512);
      const u32 o = WS_KVG + b * 14336 + w * 4096 + lane * 16;
#pragma unroll
      for (int i = 0; i < 4; i++)
        if (i < 2 || w < 3) st128(v4u{pl[i].x, pl[i].y, t0, t1}, rws, o + i * 1024);
      FENCE();
      STAMP(2);
      BAR();
      STAMP(3);
      if constexpr (MW) {
#pragma unroll
        for (int i = 0; i < 4; i++) OPQ(tp[i]);
#pragma unroll
        for (int c = 0; c < 2; c++) {
          const u32 a[8] = {tp[2 * c].x, tp[2 * c].y, tp[2 * c].z, tp[2 * c].w, tp[2 * c + 1].x, tp[2 * c + 1].y, tp[2 * c + 1].z, tp[2 * c + 1].w};
          u32 od[7];
#pragma unroll
          for (int j = 0; j < 7; j++) od[j] = __builtin_amdgcn_alignbit(a[j + 1], a[j], 16);
          const u32 base = L_TAP + c * 16640 + (pp + 1) * 16;
          *LDSP(v4u, base) = v4u{od[0], od[1], od[2], od[3]};
          *LDSP(v4u, base + 4160) = v4u{a[1], a[2], a[3], a[4]};
          *LDSP(v4u, base + 8320) = v4u{od[1], od[2], od[3], od[4]};
          *LDSP(v4u, base + 12480) = v4u{a[2], a[3], a[4], a[5]};
          if (pp == 255) {
            *LDSP(v2u, base + 16) = v2u{od[4], od[5]};
            *LDSP(v2u, base + 16 + 4160) = v2u{a[5], a[6]};
            *LDSP(v2u, base + 16 + 8320) = v2u{od[5], od[6]};
            *LDSP(v2u, base + 16 + 12480) = v2u{a[6], a[7]};
          }
        }
        if (tid < 8) *LDSP(v4u, L_TAP + tid * 4160) = v4u{0u, 0u, 0u, 0u};
      } else {
        BAR();
        const int j = b - 224;
        const rsrc_t rw = mk(j < 16 ? gw1 : gw3, D * F * 2);
        const u32 o = pp < 224 ? (u32)((pp / 14) * 28 * 3584 + (j & 15) * 224 + (pp % 14) * 16) : OOB;
#pragma unroll
        for (int i = 0; i < 28; i++) cs[i] = __builtin_amdgcn_raw_buffer_load_b128(rw, o, i * 3584, 0);
        float sm[8];
#pragma unroll
        for (int e = 0; e < 8; e++) sm[e] = 0.f;
#pragma unroll
        for (int i = 0; i < 28; i++)
#pragma unroll
          for (int e = 0; e < 4; e++) { sm[2 * e] += lo2f(cs[i][e]); sm[2 * e + 1] += hi2f(cs[i][e]); }
        *LDSP(v4f, pp * 32) = v4f{sm[0], sm[1], sm[2], sm[3]};
        *LDSP(v4f, pp * 32 + 16) = v4f{sm[4], sm[5], sm[6], sm[7]};
      }
    };
    if (w < 4) {
      if (mainwg) kvpart(std::true_type{}); else kvpart(std::false_type{});
    } else {
      {
        const u32 o = mainwg ? WS_HT + (u32)((8 * (w - 4) + (lane >> 4)) * 57344 + p * 256 + (lane & 15) * 16) : OOB;
        st128(poison, rws, o);
        st128(poison, rws, o + 4 * 57344);
      }
      FENCE();
      const rsrc_t rx = mk(gx, S * D * 2);
      const u32 so = lane < 56 ? (u32)((8 * b + 2 * (w - 4)) * 896 + lane * 16) : OOB;
      v4u sr[2];
      sr[0] = __builtin_amdgcn_raw_buffer_load_b128(rx, so, 0, 0);
      sr[1] = __builtin_amdgcn_raw_buffer_load_b128(rx, so + 896, 0, 0);
      u32 rb[8];
#pragma unroll
      for (int i = 0; i < 8; i++) rb[i] = __builtin_amdgcn_raw_buffer_load_b32(rx, colo + (i & 3) * 896, (i >> 2) * 3584, 0);
      const v2u ep = ld64(rws, WS_EPOCH, 0);
      FENCE();
      WAITV(11);
      __builtin_amdgcn_s_barrier();
      FENCE();
      const v4u r1 = mkv(gw1, D * F * 2), r3 = mkv(gw3, D * F * 2);
      const u32 wb = L_W + (w - 4) * 1024;
      const u32 wrow = (u32)((4 * (w - 4) + (lane >> 4)) * 3584 + fj * 256);
      const u32 vo1 = wrow + (((lane & 15) ^ (2 * (lane >> 4))) * 16), vo3 = vo1 ^ 128;
      USE(ep); USE(rb[7]);
      STAMP(1);
      if (mainwg) dmaw2<0, NE>(wb, vo1, vo3, r1, r3);
      float mean[2], rstd[2];
#pragma unroll
      for (int r = 0; r < 2; r++) {
        const float e[8] = {lo2f(sr[r].x), hi2f(sr[r].x), lo2f(sr[r].y), hi2f(sr[r].y), lo2f(sr[r].z), hi2f(sr[r].z), lo2f(sr[r].w), hi2f(sr[r].w)};
        const float s = ((e[0] + e[1]) + (e[2] + e[3])) + ((e[4] + e[5]) + (e[6] + e[7]));
        mean[r] = wsum(s) * (1.f / D);
        float q = 0.f;
#pragma unroll
        for (int i = 0; i < 8; i++) { const float d = e[i] - mean[r]; q += d * d; }
        q = wsum(lane < 56 ? q : 0.f);
        rstd[r] = __builtin_amdgcn_rsqf(q * (1.f / D) + 1e-5f);
      }
      if (lane == 0) *LDSP(v4f, L_STAT + (w - 4) * 16) = v4f{mean[0], rstd[0], mean[1], rstd[1]};
      if (tid == 256) *LDSP(u32, L_CNT) = 0u;
      t0 = __builtin_amdgcn_readfirstlane(ep.x) + 1u; t1 = __builtin_amdgcn_readfirstlane(ep.y) ^ 0x5bd1e995u;
      STAMP(2);
      BAR();
      STAMP(3);
      v4f sa[4];
#pragma unroll
      for (int i = 0; i < 4; i++) sa[i] = *LDSP(v4f, L_STAT + i * 16);
      const u32 xb = L_W + 12 * 8192 + (w - 4) * 1024;
#pragma unroll
      for (int h = 0; h < 2; h++) {
        const v4f a = sa[2 * h], c = sa[2 * h + 1];
        const u32 *x = rb + 4 * h;
        *LDSP(v4u, xb + (lane >> 4) * 4096 + (lane & 15) * 64 + h * 16) =
            v4u{plo(x[0], x[1]), plo(x[2], x[3]), pk((lo2f(x[0]) - a.x) * a.y, (lo2f(x[1]) - a.z) * a.w), pk((lo2f(x[2]) - c.x) * c.y, (lo2f(x[3]) - c.z) * c.w)};
        *LDSP(v4u, xb + (lane >> 4) * 4096 + (lane & 15) * 64 + 32 + h * 16) =
            v4u{phi(x[0], x[1]), phi(x[2], x[3]), pk((hi2f(x[0]) - a.x) * a.y, (hi2f(x[1]) - a.z) * a.w), pk((hi2f(x[2]) - c.x) * c.y, (hi2f(x[3]) - c.z) * c.w)};
      }
      FENCE();
      v4u px[4];
#pragma unroll
      for (int i = 0; i < 4; i++) px[i] = *LDSP(v4u, xb + i * 4096 + lane * 16);
      const u32 o = WS_XQ + b * 14336 + (w - 4) * 4096 + lane * 16;
#pragma unroll
      for (int i = 0; i < 4; i++)
        if (i < 2 || w < 7) st128(px[i], rws, o + i * 1024);
      FENCE();
      {
        const u32 ho = mainwg ? WS_HF + (u32)((rt * 8 * 56 + 4 * fj + (w - 4)) * 1024 + lane * 16) : OOB;
#pragma unroll
        for (int i = 0; i < 8; i++) st128(poison, rws, ho + i * 56 * 1024);
      }
      WAITV(8);
      STAMP(4);
      if (lane == 0) {
        const u32 old = __hip_atomic_fetch_add(LDSP(u32, L_CNT), 1u, __ATOMIC_RELAXED, __HIP_MEMORY_SCOPE_WORKGROUP);
        if (old == 3u) st64(t0, t1, rws, WS_FLAG + b * 8);
      }
      FENCE();
      if (mainwg) dmaw2<NE, 14>(wb, vo1, vo3, r1, r3);
      if (!mainwg) BAR();
      STAMP(5);
    }
  }
  PHASE();
  if (!mainwg) {
    BAR();
    if (w > 0) {
      if (HSLP) __builtin_amdgcn_s_sleep(HSLP);
      const v4u rw2 = mkv(gw2, F * D * 2);
      const int q = tid - 64, r = q / 7;
      const u32 o = (u32)(((r >> 3) * 224 + (ii - 28) * 8 + (r & 7)) * 896 + (q % 7) * 128);
#pragma unroll
      for (int t = 0; t < 7; t++) warm(o + t * 28672, rw2);
      WAITV(0);
    }
    if (w == 0) {
      v2f s = {0.f, 0.f};
#pragma unroll
      for (int g = 0; g < 16; g++) s += *LDSP(v2f, g * 448 + lane * 8);
      st128(v4u{f2u(s.x), f2u(s.y), t0, t1}, rws, lane < 56 ? WS_CS + (b - 224) * 896 + lane * 16 : OOB);
    }
    STAMP(23);
    STOUT();
    return;
  }

  if (w < 4) {
    STAMP(4);
    if (DLY1) __builtin_amdgcn_s_sleep(DLY1);
    v4u g[4];
    const u32 o = WS_KVG + (tid >> 2) * 14336 + p * 64 + (tid & 3) * 16;
#pragma unroll
    for (int i = 0; i < 4; i++) g[i] = ld128(rws, o, i * 64 * 14336);
#pragma nounroll
    for (u32 spin = 0; spin < SPIN_MAX; spin++) {
      bool bad[4], any = false;
#pragma unroll
      for (int i = 0; i < 4; i++) { bad[i] = (g[i].z != t0) | (g[i].w != t1); any |= bad[i]; }
      if (!__any(any)) break;
      __builtin_amdgcn_s_sleep(PSLP);
      FENCE();
#pragma unroll
      for (int i = 0; i < 4; i++) if (bad[i]) g[i] = ld128(rws, o, i * 64 * 14336);
    }
    STAMP(5);
    const u32 la = L_RX + ((tid >> 1) & 1) * 4608 + (508 - 8 * (tid >> 2) - 4 * (tid & 1)) * 2;
#pragma unroll
    for (int i = 0; i < 4; i++) *LDSP(v2u, la + (3 - i) * 1024) = v2u{g[i].x, g[i].y};
    if (tid < 64) *LDSP(v4u, L_RX + (tid >> 5) * 4608 + 4096 + (tid & 31) * 16) = v4u{0u, 0u, 0u, 0u};
  }
  const int ch = w >> 2, wq = w & 3;
  const float dval = lo2f(gsd[2 * p + ch]);
  const u32 fo1 = WS_FLAG + (64 * wq + lane) * 8;
  BAR();
  STAMP(6);
  v4u w13[28];
  const u32 wra = L_W + (((lane >> 4) & 1) * 4 + 2 * (lane >> 5)) * 1024 + ((lane & 15) >> 2) * 256 +
                  (((2 * w + (lane & 1)) ^ (2 * ((lane & 15) >> 2)) ^ (8 * ((lane >> 4) & 1))) * 16) + ((lane >> 1) & 1) * 8;
  {
    const v2u f1 = ld64(rws, fo1, 0);
    FENCE();
    const int la_ = lane & 15, kg = lane >> 4;
    v4f acc[8];
#pragma unroll
    for (int n = 0; n < 8; n++) acc[n] = v4f{0.f, 0.f, 0.f, 0.f};
    u32 aa = L_TAP + ch * 16640 + (la_ & 3) * 4160 + ((la_ >> 2) + 2 * kg + 16 * wq) * 8, ab = aa + 8;
    asm volatile("" : "+v"(ab));
    const u32 ba = L_RX + ch * 4608 + (30 + 8 * wq - 2 * la_ + kg) * 16;
    auto lda = [&](int o) { const v2u x = *LDSP(v2u, aa + o), y = *LDSP(v2u, ab + o); return v4u{x.x, x.y, y.x, y.y}; };
    v4u A[8], B[8], A2[8], B2[8];
#pragma unroll
    for (int m = 0; m < 8; m++) { A[m] = lda(m * 512); B[m] = *LDSP(v4u, ba + (7 - m) * 512); SCHED(); }
#pragma unroll
    for (int g = 0; g < 8; g++) {
      A2[g] = lda(64 + g * 512); B2[g] = *LDSP(v4u, ba + 64 + (7 - g) * 512);
#pragma unroll
      for (int m = 0; m <= g; m++) {
        if (m + g < 8) acc[m + g] = MFMA16(A[m], B[g], acc[m + g]);
        if (m < g && m + g < 8) acc[m + g] = MFMA16(A[g], B[m], acc[m + g]);
      }
      SCHED();
    }
    const int t = 512 * wq + 16 * la_ + 4 * kg;
    const v2u kv0 = *LDSP(v2u, L_RX + ch * 4608 + (2044 - t) * 2), kv1 = *LDSP(v2u, L_RX + ch * 4608 + (2044 - 256 - t) * 2);
    waitflag(f1, rws, fo1, t0, t1);
    WAITV(0);
    STAMP(7);
    BAR();
    STAMP(8);
    const u32 xqo = WS_XQ + (64 * wq + 2 * la_ + (kg >> 1)) * 14336 + p * 64 + ch * 32 + (kg & 1) * 16;
    const v4u xq0 = ld128(rws, xqo, 0), xq1 = ld128(rws, xqo, 32 * 14336);
    FENCE();
#pragma unroll
    for (int d = 7; d >= 0; d--) {
      if (d > 0) { w13[14 - 2 * d] = trd2<1024>(wra + (14 - 2 * d) * 8192); w13[15 - 2 * d] = trd2<1024>(wra + (15 - 2 * d) * 8192); }
#pragma unroll
      for (int m = 0; m <= d; m++) acc[d] = MFMA16(A2[m], B2[d - m], acc[d]);
      SCHED();
    }
#pragma unroll
    for (int n = 7; n >= 0; n--)
      if ((n >> 1) != wq) *LDSP(v4f, L_RED + (w * 6 + (n < 2 * wq ? n : n - 2)) * 1024 + lane * 16) = acc[n];
    v4f y0, y1;
#pragma unroll
    for (int n = 0; n < 4; n++) if (n == wq) { y0 = acc[2 * n]; y1 = acc[2 * n + 1]; }
    BAR();
    STAMP(9);
#pragma unroll
    for (int sq = 1; sq < 4; sq++) {
      const int sw = (wq + sq) & 3;
      const u32 a = L_RED + ((ch * 4 + sw) * 6 + (wq < sw ? 2 * wq : 2 * wq - 2)) * 1024 + lane * 16;
      y0 += *LDSP(v4f, a);
      y1 += *LDSP(v4f, a + 1024);
    }
    const u32 h0x = pk(lo2f(xq0.x) + lo2f(xq0.z) * (y0.x + hi2f(kv0.y) * dval), hi2f(xq0.x) + hi2f(xq0.z) * (y0.y + lo2f(kv0.y) * dval));
    const u32 h0y = pk(lo2f(xq0.y) + lo2f(xq0.w) * (y0.z + hi2f(kv0.x) * dval), hi2f(xq0.y) + hi2f(xq0.w) * (y0.w + lo2f(kv0.x) * dval));
    const u32 h1x = pk(lo2f(xq1.x) + lo2f(xq1.z) * (y1.x + hi2f(kv1.y) * dval), hi2f(xq1.x) + hi2f(xq1.z) * (y1.y + lo2f(kv1.y) * dval));
    const u32 h1y = pk(lo2f(xq1.y) + lo2f(xq1.w) * (y1.z + hi2f(kv1.x) * dval), hi2f(xq1.y) + hi2f(xq1.w) * (y1.w + lo2f(kv1.x) * dval));
    const int s1 = (wq + 1) & 3;
    const u32 tb = L_RED + ((ch * 4 + s1) * 6 + (wq < s1 ? 2 * wq : 2 * wq - 2)) * 1024;
    *LDSP(v2u, tb + la_ * 32 + kg * 8) = v2u{canon(h0x), canon(h0y)};
    *LDSP(v2u, tb + 512 + la_ * 32 + kg * 8) = v2u{canon(h1x), canon(h1y)};
    FENCE();
    st128(*LDSP(v4u, tb + lane * 16), rws, WS_HT + (u32)((8 * wq + (lane >> 3)) * 57344 + (2 * p + ch) * 128 + (lane & 7) * 16));
  }
  FENCE();
  STAMP(10);
  PHASE();

  {
    const v4u rw = mkv(w < 4 ? gw1 : gw3, D * F * 2);
    const u32 vo = (u32)((4 * wq + (lane >> 4)) * 3584 + fj * 256) + (((lane & 15) ^ (2 * (lane >> 4)) ^ (8 * ch)) * 16);
    dmaw<14, 28, 0>(L_W + w * 1024, vo, rw);
  }
  int hh = lane >> 5;
  u32 hfo = WS_HF + (u32)(((rt * 2 + ((lane >> 4) & 1)) * 4 * 56 + 4 * fj + (w >> 1)) * 1024 + ((2 * (w & 1) + hh) * 16 + (lane & 15)) * 16);
  OPQ(hfo);
  FENCE();
  if (DLY2) __builtin_amdgcn_s_sleep(DLY2);
  v4u hc[14];
  const u32 hto = WS_HT + rt * 114688 - 8192 + (tid >> 2) * 128 + (tid & 3) * 16;
  const u32 so3 = w < 4 ? 57344u : 64u;
  auto hso = [&](int i) -> u32 { const int j = i % 7; return (i / 7) * 57344u + (j < 3 ? 8192u + j * 16384u : j == 3 ? so3 : 64u + 16384u * (j - 3)); };
#pragma unroll
  for (int i = 0; i < 4; i++) hc[i] = ld128(rws, hto, hso(i));
  FENCE();
  settle<4>(hc, rws, hto, hso);
  WAITV(0);
#pragma unroll
  for (int i = 4; i < 14; i++) hc[i] = ld128(rws, hto, hso(i));
  const u32 cso = w >= 6 ? WS_CS + (u32)((w - 6) * 14336 + fj * 1024 + lane * 16) : OOB;
  v4u csl = ld128(rws, cso, 0);
  FENCE();
  STAMP(11);
  BAR();
#pragma unroll
  for (int s = 14; s < 28; s++) w13[s] = trd2<1024>(wra + (s - 14) * 8192);
#pragma unroll
  for (int i = 0; i < 3; i++) *LDSP(v4u, L_Q0 + tid * 16 + i * 8192) = hc[i];
  if (w < 4) *LDSP(v4u, L_Q0 + tid * 16 + 24576) = hc[3];
  BAR();
  STAMP(12);
  const v4u ones = {0x3F803F80u, 0x3F803F80u, 0x3F803F80u, 0x3F803F80u};
  const u32 trb = (8 * hh + ((lane & 15) >> 2)) * 64 + ((lane >> 4) & 1) * 32 + (lane & 3) * 8;
  const u32 lw = L_Q1 + 4096 + tid * 16;
  auto stats = [&](auto ns, int rg, int s0, int slot) {
    constexpr int NS = decltype(ns)::value;
    const u32 sb = (rg < 2 ? L_Q0 : L_Q1 + ((rg >> 1) - 1) * 28672) + s0 * 2048 + (8 * (lane >> 4) + ((lane & 15) >> 2)) * 64 + (rg & 1) * 32 + (lane & 3) * 8;
    v4f g = {0.f, 0.f, 0.f, 0.f}, sm = {0.f, 0.f, 0.f, 0.f};
    v4u fr[NS];
#pragma unroll
    for (int s = 0; s < NS; s++) fr[s] = trd2<256>(sb + s * 2048);
#pragma unroll
    for (int s = 0; s < NS; s++) { g = MFMA16(fr[s], fr[s], g); sm = MFMA16(ones, fr[s], sm); }
    const int i = lane & 3;
    const float sq = i == 0 ? g.x : i == 1 ? g.y : i == 2 ? g.z : g.w;
    if (((lane & 15) >> 2) == (lane >> 4)) {
      const u32 a = L_ST + (16 * rg + (lane & 15)) * 16;
      if (NS == 14) *LDSP(v4f, a) = v4f{sm.x, sq, 0.f, 0.f}; else *LDSP(v2f, a + slot * 8) = v2f{sm.x, sq};
    }
  };
  float nm, ny, cv;
  auto getst = [&](int nt) {
    const v4f s = *LDSP(v4f, L_ST + (32 * nt + (lane & 31)) * 16);
    nm = opq(s.x + s.z) * (-1.f / D);
    cv = fmaxf(__builtin_fmaf(-nm, nm, opq(s.y + s.w) * (1.f / D)), 0.f) + 1e-5f;
    ny = -1.4426950408889634f * __builtin_amdgcn_rsqf(cv);
  };
  auto epi = [&](float a, float u, float c1, float c3) -> float {
    const float g = opq(__builtin_fmaf(nm, c1, a)), uu = opq(__builtin_fmaf(nm, c3, u));
    const float e = __builtin_amdgcn_exp2f(opq(g * ny));
    return opq(opq(g * uu) * __builtin_amdgcn_rcpf(opq(__builtin_fmaf(e, cv, cv))));
  };
  if (w < 4) stats(std::integral_constant<int, 7>{}, w >> 1, 7 * (w & 1), w & 1);
  SCHED();
  const v4u rw2 = mkv(gw2, F * D * 2);
  u32 w2o = (u32)((224 * w + (lane >> 3)) * 896 + dt * 128 + ((lane & 7) ^ ((lane >> 3) & 2)) * 16);
  u32 w2a = (u32)((lane >> 4) * 1024 + ((lane & 15) >> 2) * 128 + (((lane >> 3) & 1) | ((lane >> 3) & 2)) * 32 + (lane & 3) * 8);
  OPQ(w2o); OPQ(w2a);
  const u32 slA = w < 7 ? 86016u + w * 4096u : 147456u, slB = w < 7 ? L_Q0 + w * 4096u : 151552u, slC = w < 7 ? L_Q1 + w * 4096u : 155648u;
  v4u wa[7][4];
  auto rdw = [&](int t, u32 sl) {
    const u32 a = w2a + sl;
#pragma unroll
    for (int j = 0; j < 4; j++) wa[t][j] = trd2<512>(a ^ (32u * j));
  };
  auto usew = [&](int t) { asm volatile("" ::"v"(wa[t][0]), "v"(wa[t][1]), "v"(wa[t][2]), "v"(wa[t][3])); };
  auto rdres = [&]() {
    return trd2<256>(((w >> 1) == 0 ? L_Q0 : L_Q1 + ((w >> 1) - 1) * 28672) + (64 * dt + 16 * (lane >> 4) + 8 * ((lane >> 1) & 1) + ((lane & 15) >> 2)) * 64 + 32 * q2 + 16 * (w & 1) + 8 * (lane & 1));
  };
  const u32 h2o = WS_HF + (u32)(((rt * 2 + q2) * 4 * 56 + 7 * w) * 1024 + lane * 16);
  const u32 csa = L_CSV + (16 * w + 8 * hh) * 4;
  auto lin = [](int i) -> u32 { return i * 1024u; };
  v4u hf[4][7], res;
  v16f ac[4];
#define PDX 4
  constexpr int PD = PDX;
#pragma unroll
  for (int nt = 0; nt < 4; nt++) {
    u32 tb_ = trb + (nt == 0 ? L_Q0 : L_Q1 + (nt - 1) * 28672);
    OPQ(tb_);
    v4u bfr[28];
#pragma unroll
    for (int s = 0; s < PD; s++) bfr[s] = trd2<256>(tb_ + s * 1024);
    float hv[8];
    v4f c1, c3;
    if (nt > 0) getst(nt - 1);
#pragma unroll
    for (int r = 0; r < 16; r++) ac[nt][r] = 0.f;
    SCHED();
#pragma unroll
    for (int s = 0; s < 28; s++) {
      if (s + PD < 28) bfr[s + PD] = trd2<256>(tb_ + (s + PD) * 1024);
      if (nt > 0 && (s == 0 || s == 9)) { c1 = *LDSP(v4f, csa + (s ? 16 : 0)); c3 = *LDSP(v4f, csa + 512 + (s ? 16 : 0)); }
      ac[nt] = MFMA32(w13[s], bfr[s], ac[nt]);
      if (nt == 0) {
        if (s == 8) settle<3>(hc + 4, rws, hto, [&](int i) { return hso(i + 4); });
        if (s == 7 && w >= 4) *LDSP(v4u, L_Q1 + (tid & 255) * 16) = hc[3];
        if (s >= 8 && s < 11) *LDSP(v4u, lw + (s - 8) * 8192) = hc[s - 4];
        if (s == 15) settle<4>(hc + 7, rws, hto, [&](int i) { return hso(i + 7); });
        if (s >= 15 && s < 19) *LDSP(v4u, lw + (s - 12) * 8192) = hc[s - 8];
        if (s == 22) settle<3>(hc + 11, rws, hto, [&](int i) { return hso(i + 11); });
        if (s >= 22 && s < 25) *LDSP(v4u, lw + (s - 15) * 8192) = hc[s - 11];
      } else {
        if (s >= 2 && s < 18 && (s & 1) == 0) { const int r = (s - 2) >> 1; hv[r] = epi(ac[nt - 1][r], ac[nt - 1][r + 8], c1[r & 3], c3[r & 3]); }
        if (s == 18) st128(v4u{canon(pk(hv[0], hv[1])), canon(pk(hv[2], hv[3])), canon(pk(hv[4], hv[5])), canon(pk(hv[6], hv[7]))}, rws, hfo);
        if (s == 19) { hfo += 56 * 1024; OPQ(hfo); }
      }
      if (nt == 0 && s == 25) {
        if (w >= 6) {
          if (UNLIKELY(__any((csl.z != t0) | (csl.w != t1)))) {
#pragma nounroll
            for (u32 spin = 0; spin < SPIN_MAX; spin++) {
              FENCE();
              csl = ld128(rws, cso, 0);
              if (!__any((csl.z != t0) | (csl.w != t1))) break;
            }
            DRAINV();
          }
          *LDSP(v2u, L_CSV + (tid - 384) * 8) = v2u{csl.x, csl.y};
        }
        dmak<0>(slA, w2o, rw2);
      }
      if (nt == 1) {
        if (s == 0) dmak<1>(slB, w2o, rw2);
        if (s == 6) { WAITV(4); rdw(0, slA); }
        if (s == 9) { usew(0); dmak<2>(slA, w2o, rw2); }
        if (s == 16) { WAITV(4); rdw(1, slB); }
        if (s == 19) { usew(1); dmak<3>(slB, w2o, rw2); }
      }
      if (nt == 2) {
        if (s == 0) dmak<4>(slC, w2o, rw2);
        if (s == 4) { WAITV(9); rdw(2, slA); }
        if (s == 7) { usew(2); dmak<5>(slA, w2o, rw2); }
      }
      if (nt == 3) {
        if (s == 4) { WAITV(1); rdw(5, slA); }
        if (s == 7) { usew(5); dmak<6>(slA, w2o, rw2); }
        if (s == 8) rdw(3, slB);
        if (s == 12) rdw(4, slC);
        if (s >= 13 && s < 20) hf[0][s - 13] = ld128(rws, h2o, (s - 13) * 1024);
        if (s >= 20 && s < 27) hf[1][s - 20] = ld128(rws, h2o, (56 + s - 20) * 1024);
      }
      SCHED();
    }
    STAMP(13 + nt);
    if (nt == 0) {
      if (w < 2) res = rdres();
      BAR();
      if (w >= 2) res = rdres();
      if (w < 4) stats(std::integral_constant<int, 7>{}, 2 + (w >> 1), 7 * (w & 1), w & 1);
      else stats(std::integral_constant<int, 14>{}, w, 0, 0);
    }
    if (nt == 1) BAR();
    SCHED();
  }
  {
    getst(3);
    float hv[8];
#pragma unroll
    for (int h = 0; h < 2; h++) {
      const v4f c1 = *LDSP(v4f, csa + 16 * h), c3 = *LDSP(v4f, csa + 512 + 16 * h);
#pragma unroll
      for (int r = 0; r < 4; r++) hv[4 * h + r] = epi(ac[3][4 * h + r], ac[3][4 * h + r + 8], c1[r], c3[r]);
    }
    st128(v4u{canon(pk(hv[0], hv[1])), canon(pk(hv[2], hv[3])), canon(pk(hv[4], hv[5])), canon(pk(hv[6], hv[7]))}, rws, hfo);
  }
  FENCE();
#pragma unroll
  for (int s = 0; s < 7; s++) hf[2][s] = ld128(rws, h2o, (2 * 56 + s) * 1024);
  FENCE();
  WAITV(23);
  rdw(6, slA);
  STAMP(17);
  PHASE();
  const u32 pw = (u32)(w * 4096 + lane * 16);
  auto finalize = [&]() {
    const int n = 8 * (w & 1) + (lane & 7);
    const u32 a = (u32)((w >> 1) * 32768 + (lane >> 3) * 512 + (n ^ ((lane >> 1) & 8)) * 16);
    v4f e = *LDSP(v4f, a), o = *LDSP(v4f, a + 256);
#pragma unroll
    for (int u = 1; u < 8; u++) { e += *LDSP(v4f, a + u * 4096); o += *LDSP(v4f, a + u * 4096 + 256); }
    const v4u ov = {pk(lo2f(res.x) + e.x, hi2f(res.x) + e.y), pk(lo2f(res.y) + e.z, hi2f(res.y) + e.w),
                    pk(lo2f(res.z) + o.x, hi2f(res.z) + o.y), pk(lo2f(res.w) + o.z, hi2f(res.w) + o.w)};
    *(v4u*)(gout + (128 * rt + 32 * (w >> 1) + 16 * q2 + n) * D + 64 * dt + 8 * (lane >> 3)) = ov;
  };
#pragma unroll
  for (int g = 0; g < 4; g++) {
    if (g == 3) {
      BAR();
      STAMP(22);
      if (w < 6) finalize();
      FENCE();
    }
    settle<7>(hf[g], rws, h2o + g * 56 * 1024, lin);
    STAMP(18 + g);
    v4f c[4];
#pragma unroll
    for (int i = 0; i < 4; i++) c[i] = v4f{0.f, 0.f, 0.f, 0.f};
#pragma unroll
    for (int s = 0; s < 7; s++)
#pragma unroll
      for (int i = 0; i < 4; i++) c[i] = MFMA16(wa[s][i], hf[g][s], c[i]);
    SCHED();
    if (g == 0) BAR();
    if (g == 1) {
#pragma unroll
      for (int s = 0; s < 7; s++) hf[3][s] = ld128(rws, h2o, (3 * 56 + s) * 1024);
      FENCE();
    }
#pragma unroll
    for (int i = 0; i < 4; i++) *LDSP(v4f, g * 32768 + (pw ^ (128u * (i & 1))) + i * 1024) = c[i];
    SCHED();
  }
  BAR();
  if (w >= 6) finalize();
  if (b == 0 && tid == 0) st64(t0, t1 ^ 0x5bd1e995u, rws, WS_EPOCH);
  STAMP(23);
  STOUT();
}

__global__ void __launch_bounds__(64) spink(unsigned char* p) {
  u64 t0_, t_; u32 n_ = 0;
  asm volatile("s_memrealtime %0\n\ts_waitcnt lgkmcnt(0)" : "=s"(t0_)::"memory");

  do { asm volatile("s_memrealtime %0\n\ts_waitcnt lgkmcnt(0)" : "=s"(t_)::"memory"); } while ((u32)(t_ - t0_) < 1000u && ++n_ < 100000u);
}

extern "C" void kernel_launch(void* const* d_in, const int* in_sizes, int n_in, void* d_out, int out_size, void* d_ws, size_t ws_size,
                              hipStream_t stream) {
  (void)hipFuncSetAttribute((const void*)fused_kernel, hipFuncAttributeMaxDynamicSharedMemorySize, L_TOTAL);
  fused_kernel<<<256, 512, L_TOTAL, stream>>>((const u16*)d_in[0], (const u16*)d_in[1], (const u16*)d_in[2], (const u16*)d_in[3],
                                              (const u16*)d_in[4], (const u16*)d_in[5], (const u16*)d_in[7], (const u16*)d_in[8],
                                              (const u16*)d_in[9], (u16*)d_out, (unsigned char*)d_ws);
  (void)hipFuncSetAttribute((const void*)spink, hipFuncAttributeMaxDynamicSharedMemorySize, L_TOTAL);
  spink<<<1, 64, 0, stream>>>((unsigned char*)d_ws);
}
```

```cpp
#include <hip/hip_runtime.h>
#include <hip/hip_bf16.h>
#include <type_traits>

typedef unsigned u32;
typedef unsigned short u16;
typedef unsigned long long u64;
typedef u32 v4u __attribute__((ext_vector_type(4)));
typedef u32 v2u __attribute__((ext_vector_type(2)));
typedef float v4f __attribute__((ext_vector_type(4)));
typedef float v2f __attribute__((ext_vector_type(2)));
typedef float v16f __attribute__((ext_vector_type(16)));
typedef __bf16 bf8 __attribute__((ext_vector_type(8)));
typedef __bf16 bf2 __attribute__((ext_vector_type(2)));
typedef short s4 __attribute__((ext_vector_type(4)));
typedef __amdgpu_buffer_rsrc_t rsrc_t;
#define LDSP(T, off) ((__attribute__((address_space(3))) T*)(size_t)(off))

constexpr int S = 2048, D = 448, F = 1792;
constexpr u32 WS_EPOCH = 0;
constexpr u32 WS_FB = 4096;
constexpr u32 WS_CS = 16384;
constexpr u32 WS_SG = 49152;
constexpr u32 WS_KVG = 81920;
constexpr u32 WS_XG = WS_KVG + 256u * 224 * 64;
constexpr u32 WS_HT = WS_XG + 256u * 224 * 64;
constexpr u32 WS_HF = WS_HT + 32u * 448 * 128;
constexpr u32 WS_END = WS_HF + 128u * 56 * 1024;
constexpr u32 WS_DBG = WS_END;
constexpr u32 L_W = 0;
constexpr u32 TAPC = 4416, RXC = 4352;
constexpr u32 L_TAP = 114688;
constexpr u32 L_RX = L_TAP + 8 * TAPC;
constexpr u32 L_RED0 = 81920, L_SX0 = 106496, L_RED1 = 114688, L_SX1 = 139264;
constexpr u32 L_H = 57344;
constexpr u32 L_SA = 114688;
constexpr u32 L_ST = 147456;
constexpr u32 L_CSV = 149504;
constexpr u32 L_SC = 151552;
constexpr u32 L_DUM = 159744;
constexpr u32 L_CNT = 163328;
constexpr u32 L_TOTAL = 160 * 1024;
constexpr u32 OOB = 0x80000000u;
constexpr int SC1 = 16;
constexpr u32 SPIN_MAX = 1u << 16;

#define FENCE() asm volatile("" ::: "memory")
#define WAITV(n) asm volatile("s_waitcnt vmcnt(" #n ")" ::: "memory")
#define DRAINV() __builtin_amdgcn_s_waitcnt(0x0F70)
#define BAR() do { asm volatile("s_waitcnt lgkmcnt(0)" ::: "memory"); __builtin_amdgcn_s_barrier(); FENCE(); } while (0)
#define SCHED() __builtin_amdgcn_sched_barrier(0)
#define PHASE() do { SCHED(); asm volatile("" : "+v"(tid)); lane = tid & 63; SCHED(); } while (0)
#define UNLIKELY(x) __builtin_expect(!!(x), 0)
#define USE(x) asm volatile("" ::"v"(x))
#define OPQ(x) asm volatile("" : "+v"(x))
#define IC(n) std::integral_constant<int, n>{}

template <int I, int N, class Fn> __device__ __forceinline__ void sfor(Fn&& f) {
  if constexpr (I < N) { f(std::integral_constant<int, I>{}); sfor<I + 1, N>(f); }
}
template <int N> __device__ __forceinline__ void waitv() { asm volatile("s_waitcnt vmcnt(%0)" ::"n"(N) : "memory"); }
template <int N, class T> __device__ __forceinline__ void waitv(T& a) { asm volatile("s_waitcnt vmcnt(%1)" : "+v"(a) : "n"(N) : "memory"); }
__device__ __forceinline__ rsrc_t mk(const void* p, u32 bytes) { return __builtin_amdgcn_make_buffer_rsrc((void*)p, 0, bytes, 0x00020000); }
__device__ __forceinline__ v4u mkv(const void* p, u32 bytes) { const u64 a = (u64)p; return v4u{(u32)a, (u32)(a >> 32) & 0xffffu, bytes, 0x00020000u}; }
__device__ __forceinline__ float lo2f(u32 v) { return __builtin_bit_cast(float, v << 16); }
__device__ __forceinline__ float hi2f(u32 v) { return __builtin_bit_cast(float, v & 0xffff0000u); }
__device__ __forceinline__ u32 pk(float a, float b) { return __builtin_bit_cast(u32, __builtin_convertvector(v2f{a, b}, bf2)); }
__device__ __forceinline__ u32 plo(u32 a, u32 b) { return __builtin_amdgcn_perm(b, a, 0x05040100u); }
__device__ __forceinline__ u32 phi(u32 a, u32 b) { return __builtin_amdgcn_perm(b, a, 0x07060302u); }
__device__ __forceinline__ u32 canon(u32 v) { return min(v, 0xFFFFFFFEu); }
__device__ __forceinline__ u32 max4(v4u t) { return max(max(t.x, t.y), max(t.z, t.w)); }
__device__ __forceinline__ u32 max5(u32 m, v4u t) { return max(max(max(m, t.x), t.y), max(t.z, t.w)); }
__device__ __forceinline__ u32 swz(u32 a) { return a ^ ((a >> 3) & 32u); }
__device__ __forceinline__ bf8 asbf(v4u v) { return __builtin_bit_cast(bf8, v); }
__device__ __forceinline__ v2u trd(u32 a) { return __builtin_bit_cast(v2u, __builtin_amdgcn_ds_read_tr16_b64_v4i16(LDSP(s4, a))); }
template <int O> __device__ __forceinline__ v4u trd2(u32 a) { const v2u x = trd(a), y = trd(a + O); return v4u{x.x, x.y, y.x, y.y}; }
__device__ __forceinline__ void st128(v4u v, rsrc_t r, u32 off) { __builtin_amdgcn_raw_buffer_store_b128(v, r, off, 0, SC1); }
__device__ __forceinline__ void st64(u32 a, u32 b_, rsrc_t r, u32 off) { __builtin_amdgcn_raw_buffer_store_b64(v2u{a, b_}, r, off, 0, SC1); }
__device__ __forceinline__ v4u ld128(rsrc_t r, u32 off, u32 so) { return __builtin_amdgcn_raw_buffer_load_b128(r, off, so, SC1); }
__device__ __forceinline__ v2u ld64(rsrc_t r, u32 off, u32 so) { return __builtin_amdgcn_raw_buffer_load_b64(r, off, so, SC1); }
__device__ __forceinline__ u32 f2u(float v) { return __builtin_bit_cast(u32, v); }
__device__ __forceinline__ float u2f(u32 v) { return __builtin_bit_cast(float, v); }
__device__ __forceinline__ float opq(float v) { asm("" : "+v"(v)); return v; }
template <int CTRL> __device__ __forceinline__ float dppadd(float v) { return v + u2f(__builtin_amdgcn_update_dpp(0, f2u(v), CTRL, 0xF, 0xF, true)); }
__device__ __forceinline__ float swap16add(float a, float b) { const auto r = __builtin_amdgcn_permlane16_swap(f2u(a), f2u(b), false, false); return u2f(r[0]) + u2f(r[1]); }
__device__ __forceinline__ float swap32add(float a, float b) { const auto r = __builtin_amdgcn_permlane32_swap(f2u(a), f2u(b), false, false); return u2f(r[0]) + u2f(r[1]); }
__device__ __forceinline__ float wsum(float v) {
  v = dppadd<0xB1>(v); v = dppadd<0x4E>(v); v = dppadd<0x141>(v); v = dppadd<0x140>(v);
  v = swap16add(v, v);
  return swap32add(v, v);
}
template <int OFF> __device__ __forceinline__ void ald32(u32& d, u32 vo, v4u r, u32 so) {
  asm volatile("buffer_load_dword %0, %1, %2, %3 offen offset:%4" : "=v"(d) : "v"(vo), "s"(r), "s"(so), "n"(OFF) : "memory");
}
template <int OFF> __device__ __forceinline__ void ald128(v4u& d, u32 vo, v4u r) {
  asm volatile("buffer_load_dwordx4 %0, %1, %2, 0 offen offset:%3" : "=v"(d) : "v"(vo), "s"(r), "n"(OFF) : "memory");
}
__device__ __forceinline__ void ald128s(v4u& d, u32 vo, v4u r) { asm volatile("buffer_load_dwordx4 %0, %1, %2, 0 offen sc1" : "=v"(d) : "v"(vo), "s"(r) : "memory"); }
__device__ __forceinline__ void ald64s(v2u& d, u32 vo, v4u r) { asm volatile("buffer_load_dwordx2 %0, %1, %2, 0 offen sc1" : "=v"(d) : "v"(vo), "s"(r) : "memory"); }
template <u32 LI, u32 SO> __device__ __forceinline__ void dma16(u32 lb, u32 vo, v4u r) {
  u32 t;
  asm volatile("s_add_u32 m0, %1, %4\n\ts_mov_b32 %0, %5\n\tbuffer_load_dwordx4 %2, %3, %0 offen lds"
               : "=&s"(t) : "s"(lb), "v"(vo), "s"(r), "n"(LI), "n"(SO) : "memory", "scc");
}
template <u32 LI, u32 SO> __device__ __forceinline__ void dma16s(u32 lb, u32 vo, v4u r) {
  u32 t;
  asm volatile("s_add_u32 m0, %1, %4\n\ts_mov_b32 %0, %5\n\tbuffer_load_dwordx4 %2, %3, %0 offen sc1 lds"
               : "=&s"(t) : "s"(lb), "v"(vo), "s"(r), "n"(LI), "n"(SO) : "memory", "scc");
}
__device__ __forceinline__ void warm(u32 vo, v4u r) {
  asm volatile("s_mov_b32 m0, %2\n\ts_nop 0\n\tbuffer_load_dword %0, %1, 0 offen lds" ::"v"(vo), "s"(r), "n"(L_DUM) : "memory");
}
template <int S0, int S1> __device__ __forceinline__ void dmaw2(u32 lb, u32 vo1, u32 vo3, v4u r1, v4u r3) {
  if constexpr (S0 < S1) {
    dma16<(S0 % 14) * 8192u, S0 * 57344u>(lb, vo1, r1); dma16<(S0 % 14) * 8192u + 4096u, S0 * 57344u>(lb, vo3, r3);
    dmaw2<S0 + 1, S1>(lb, vo1, vo3, r1, r3);
  }
}
__device__ __forceinline__ void lsig(u32 a, int lane) {
  FENCE();
  if (lane == 0) (void)__hip_atomic_fetch_add(LDSP(u32, a), 1u, __ATOMIC_RELAXED, __HIP_MEMORY_SCOPE_WORKGROUP);
  FENCE();
}
__device__ __forceinline__ void lwait(u32 a) {
#pragma nounroll
  for (u32 spin = 0; spin < SPIN_MAX * 16; spin++) {
    FENCE();
    if (__builtin_amdgcn_readfirstlane(*LDSP(volatile u32, a)) >= 8u) break;
    __builtin_amdgcn_s_sleep(1);
  }
  FENCE();
}
#define MFMA32(a, b, c) __builtin_amdgcn_mfma_f32_32x32x16_bf16(asbf(a), asbf(b), c, 0, 0, 0)
#define MFMA16(a, b, c) __builtin_amdgcn_mfma_f32_16x16x32_bf16(asbf(a), asbf(b), c, 0, 0, 0)
#define STAMP(i)
#define STOUT()
#define PSLP 4
#define NE 4
#define HSLP 0
#define RSLP 0
#define NPRE 3

template <int N, class SOF> __device__ __forceinline__ void settle(v4u* t, rsrc_t r, u32 off, SOF so) {
  u32 m = 0;
#pragma unroll
  for (int i = 0; i < N; i++) m = max5(m, t[i]);
  if (UNLIKELY(__any(m == 0xFFFFFFFFu))) {
#pragma nounroll
    for (u32 spin = 0; spin < SPIN_MAX; spin++) {
      bool any = false;
      FENCE();
#pragma unroll
      for (int i = 0; i < N; i++)
        if (__any(max4(t[i]) == 0xFFFFFFFFu)) { t[i] = ld128(r, off, so(i)); any = true; }
      if (!any) break;
    }
    DRAINV();
  }
}
__device__ __forceinline__ void fixg(v4u& g, rsrc_t r, u32 off, u32 t0, u32 t1) {
  if (UNLIKELY(__any((g.z != t0) | (g.w != t1)))) {
#pragma nounroll
    for (u32 spin = 0; spin < SPIN_MAX; spin++) {
      FENCE();
      g = ld128(r, off, 0);
      if (!__any((g.z != t0) | (g.w != t1))) break;
    }
    DRAINV();
  }
}

constexpr int T_CS = 0, T_FL = 54, T_FB = 76;
constexpr int T_PO[4] = {8, 9, 11, 13};
constexpr int T_W[7][4] = {{1, 3, 5, 7}, {17, 19, 21, 23}, {34, 36, 38, 40}, {51, 53, 55, 57}, {59, 61, 63, 65}, {67, 69, 71, 73}, {93, 95, 97, 99}};
constexpr int T_RD[6] = {14, 31, 48, 90, 99, 103};
constexpr int T_HD[7] = {18, 20, 22, 24, 26, 28, 30};
constexpr int T_HV = 42;
constexpr int T_ST[3] = {46, 74, 102};
constexpr int T_HF[14] = {85, 86, 87, 92, 94, 96, 98, 104, 105, 106, 107, 108, 109, 110};
constexpr int nvm(int a, int b) {
  int n = 0;
  auto in = [&](int t) { if (t > a && t < b) n++; };
  in(T_CS); in(T_FB); in(T_FL);
  for (int i = 0; i < 4; i++) in(T_PO[i]);
  for (int i = 0; i < 7; i++) { in(T_HD[i]); for (int j = 0; j < 4; j++) in(T_W[i][j]); }
  for (int i = 0; i < 3; i++) in(T_ST[i]);
  for (int i = 0; i < 14; i++) in(T_HF[i]);
  return n;
}
template <int N> constexpr int find(const int (&a)[N], int t) { for (int i = 0; i < N; i++) if (a[i] == t) return i; return -1; }
constexpr int findw(int t) { for (int i = 0; i < 28; i++) if (T_W[i / 4][i % 4] == t) return i; return -1; }

__global__ void __launch_bounds__(512, 2)
fused_kernel(const u16* __restrict__ gx, const u16* __restrict__ gk, const u16* __restrict__ gv, const u16* __restrict__ gsk,
             const u16* __restrict__ gssm, const u16* __restrict__ gsd, const u16* __restrict__ gw1, const u16* __restrict__ gw3,
             const u16* __restrict__ gw2, u16* __restrict__ gout, unsigned char* __restrict__ ws) {
  asm volatile("" ::"s"(gx), "s"(gk), "s"(gv), "s"(gsk), "s"(gssm), "s"(gsd), "s"(gw1), "s"(gw3), "s"(gw2), "s"(gout), "s"(ws));
  int tid = threadIdx.x, lane = tid & 63;
  const int w = __builtin_amdgcn_readfirstlane(tid >> 6), b = blockIdx.x;
  const int xc = b & 7, ii = b >> 3;
  const bool mainwg = ii < 28;
  const int p = xc * 28 + ii;
  const int dt = ii % 7, q2 = xc & 1;
  const int rt = 4 * (xc >> 1) + ii / 7, fj = 7 * q2 + dt;
  const rsrc_t rws = mk(ws, WS_END);
  const v4u rwv = mkv(ws, WS_END);
  const v4u poison = {0xFFFFFFFFu, 0xFFFFFFFFu, 0xFFFFFFFFu, 0xFFFFFFFFu};
  STAMP(0);
  const v2u ep = *(const __attribute__((address_space(4))) v2u*)(ws + WS_EPOCH);
  const u32 dv2 = *(const __attribute__((address_space(4))) u32*)(gsd + 2 * (mainwg ? p : 0));
  u32 t0, t1;
  auto tags = [&]() { t0 = ep.x + 1u; t1 = ep.y ^ 0x5bd1e995u; asm volatile("" : "+s"(t0), "+s"(t1)); };

  v4u g[4], xg;
  const int ch = w >> 2, wq = w & 3;
  const int tl = 256 * wq + 128 * (lane >> 5) + 8 * (lane & 15) + 4 * ((lane >> 4) & 1);
  u32 kvo = w < 4 ? WS_KVG + (tid >> 2) * 14336 + p * 64 + (tid & 3) * 16 : WS_SG + (tid - 256) * 16;
  u32 xo = WS_XG + (u32)((tl >> 3) * 14336 + p * 64 + ch * 32 + ((lane >> 4) & 1) * 16);
  OPQ(kvo); OPQ(xo);
  {
    const int pp = tid & 255;
    const u32 colo = pp < 224 ? (u32)(8 * b * 896 + pp * 4) : OOB;
    auto kvpart = [&](auto mw) {
      constexpr bool MW = decltype(mw)::value;
      u32 ra[11], rb[8];
      const rsrc_t rk = mk(gk, S * D * 2), rv = mk(gv, S * D * 2);
      const u32 halo = (b > 0 && pp < 224) ? colo - 3 * 896 : OOB;
#pragma unroll
      for (int i = 0; i < 3; i++) ra[i] = __builtin_amdgcn_raw_buffer_load_b32(rk, halo + i * 896, 0, 0);
#pragma unroll
      for (int i = 0; i < 8; i++) ra[3 + i] = __builtin_amdgcn_raw_buffer_load_b32(rk, colo + (i & 3) * 896, (i >> 2) * 3584, 0);
      const v4u skw = __builtin_amdgcn_raw_buffer_load_b128(mk(gsk, D * 8), pp * 16, 0, 0);
#pragma unroll
      for (int i = 0; i < 8; i++) rb[i] = __builtin_amdgcn_raw_buffer_load_b32(rv, colo + (i & 3) * 896, (i >> 2) * 3584, 0);
      FENCE();
      v4u tp[4], cs[28];
      if constexpr (MW) {
        const rsrc_t rt_ = mk(gssm, D * S * 2);
        const u32 o = (u32)(2 * p * 4096 + pp * 16);
#pragma unroll
        for (int c = 0; c < 2; c++) {
          tp[2 * c] = __builtin_amdgcn_raw_buffer_load_b128(rt_, pp > 0 ? o - 16 : OOB, c * 4096, 0);
          tp[2 * c + 1] = __builtin_amdgcn_raw_buffer_load_b128(rt_, o, c * 4096, 0);
        }
        if (tid == 0) *LDSP(v4u, L_CNT) = v4u{0u, 0u, 0u, 0u};
        BAR();
      }
      USE(rb[7]);
      STAMP(1);
      const float s0[4] = {lo2f(skw.x), hi2f(skw.x), lo2f(skw.y), hi2f(skw.y)};
      const float s1[4] = {lo2f(skw.z), hi2f(skw.z), lo2f(skw.w), hi2f(skw.w)};
      float k0[8], k1[8];
#pragma unroll
      for (int i = 0; i < 8; i++) {
        float a0 = lo2f(ra[i + 3]) * s0[0], a1 = hi2f(ra[i + 3]) * s1[0];
#pragma unroll
        for (int j = 1; j < 4; j++) { a0 += lo2f(ra[i + 3 - j]) * s0[j]; a1 += hi2f(ra[i + 3 - j]) * s1[j]; }
        k0[i] = a0 * lo2f(rb[i]); k1[i] = a1 * hi2f(rb[i]);
      }
      const u32 sc = L_TAP + 256 + w * 1024;
      *LDSP(v4u, sc + (lane >> 5) * TAPC + (lane & 31) * 32) = v4u{pk(k0[3], k0[2]), pk(k0[1], k0[0]), pk(k0[7], k0[6]), pk(k0[5], k0[4])};
      *LDSP(v4u, sc + (lane >> 5) * TAPC + (lane & 31) * 32 + 16) = v4u{pk(k1[3], k1[2]), pk(k1[1], k1[0]), pk(k1[7], k1[6]), pk(k1[5], k1[4])};
      tags();
      FENCE();
      v2u pl[4];
#pragma unroll
      for (int i = 0; i < 4; i++) pl[i] = *LDSP(v2u, sc + lane * 8 + (i >> 1) * TAPC + (i & 1) * 512);
      const u32 o = WS_KVG + b * 14336 + w * 4096 + lane * 16;
#pragma unroll
      for (int i = 0; i < 4; i++)
        if (i < 2 || w < 3) st128(v4u{pl[i].x, pl[i].y, t0, t1}, rws, o + i * 1024);
      FENCE();
      STAMP(2);
      if constexpr (MW) {
#pragma unroll
        for (int i = 0; i < 4; i++) OPQ(tp[i]);
#pragma unroll
        for (int c = 0; c < 2; c++) {
          const u32 a[8] = {tp[2 * c].x, tp[2 * c].y, tp[2 * c].z, tp[2 * c].w, tp[2 * c + 1].x, tp[2 * c + 1].y, tp[2 * c + 1].z, tp[2 * c + 1].w};
          u32 od[7];
#pragma unroll
          for (int j = 0; j < 7; j++) od[j] = __builtin_amdgcn_alignbit(a[j + 1], a[j], 16);
          const u32 base = L_TAP + c * 4 * TAPC + swz((pp + 16) * 16);
          *LDSP(v4u, base) = v4u{od[0], od[1], od[2], od[3]};
          *LDSP(v4u, base + TAPC) = v4u{a[1], a[2], a[3], a[4]};
          *LDSP(v4u, base + 2 * TAPC) = v4u{od[1], od[2], od[3], od[4]};
          *LDSP(v4u, base + 3 * TAPC) = v4u{a[2], a[3], a[4], a[5]};
          if (pp == 255) {
            const u32 e = L_TAP + c * 4 * TAPC + 4384;
            *LDSP(v2u, e) = v2u{od[4], od[5]};
            *LDSP(v2u, e + TAPC) = v2u{a[5], a[6]};
            *LDSP(v2u, e + 2 * TAPC) = v2u{od[5], od[6]};
            *LDSP(v2u, e + 3 * TAPC) = v2u{a[6], a[7]};
          }
        }
        if (tid < 128) *LDSP(v4u, L_TAP + (tid >> 4) * TAPC + (tid & 15) * 16) = v4u{0u, 0u, 0u, 0u};
        lsig(L_CNT, lane);
        STAMP(3);
#pragma unroll
        for (int i = 0; i < 4; i++) g[i] = ld128(rws, kvo, i * 64 * 14336);
        xg = ld128(rws, xo, 0);
        FENCE();
      } else {
        const int j = b - 224;
        const rsrc_t rw = mk(j < 16 ? gw1 : gw3, D * F * 2);
        u32 o = pp < 224 ? (u32)((pp / 14) * 28 * 3584 + (j & 15) * 224 + (pp % 14) * 16) : OOB;
#pragma unroll
        for (int i = 0; i < 28; i++) cs[i] = __builtin_amdgcn_raw_buffer_load_b128(rw, o, i * 3584, 0);
        float sm[8];
#pragma unroll
        for (int e = 0; e < 8; e++) sm[e] = 0.f;
#pragma unroll
        for (int i = 0; i < 28; i++)
#pragma unroll
          for (int e = 0; e < 4; e++) { sm[2 * e] += lo2f(cs[i][e]); sm[2 * e + 1] += hi2f(cs[i][e]); }
        *LDSP(v4f, pp * 32) = v4f{sm[0], sm[1], sm[2], sm[3]};
        *LDSP(v4f, pp * 32 + 16) = v4f{sm[4], sm[5], sm[6], sm[7]};
        STAMP(3);
        BAR();
        if (w == 0) {
          v2f s = {0.f, 0.f};
#pragma unroll
          for (int q = 0; q < 16; q++) s += *LDSP(v2f, q * 448 + lane * 8);
          st128(v4u{f2u(s.x), f2u(s.y), t0, t1}, rws, lane < 56 ? WS_CS + j * 896 + lane * 16 : OOB);
        }
      }
    };
    auto xpart = [&](auto mw) {
      constexpr bool MW = decltype(mw)::value;
      const v4u rx = mkv(gx, S * D * 2);
      const u32 so = lane < 56 ? (u32)((8 * b + 2 * (w - 4)) * 896 + lane * 16) : OOB;
      v4u sr0, sr1;
      u32 rb[8];
      ald128<0>(sr0, so, rx); ald128<896>(sr1, so, rx);
      sfor<0, 8>([&](auto I) { constexpr int i = I; ald32<(i & 3) * 896>(rb[i], colo, rx, (i >> 2) * 3584); });
      const v4u r1 = mkv(gw1, D * F * 2), r3 = mkv(gw3, D * F * 2);
      const u32 wb = L_W + (w - 4) * 1024;
      const u32 vo1 = (u32)((4 * (w - 4) + (lane >> 4)) * 3584 + fj * 256) + (((lane & 15) ^ (2 * (lane >> 4))) * 16), vo3 = vo1 ^ 128;
      {
        const u32 o = WS_HT + (u32)(b * 7168 + (w - 4) * 1792 + lane * 16);
        st128(poison, rws, o);
        st128(poison, rws, lane < 48 ? o + 1024 : OOB);
        FENCE();
      }
      if constexpr (MW) {
        __builtin_amdgcn_s_barrier();
        if (RSLP) __builtin_amdgcn_s_sleep(RSLP);
        dmaw2<0, NE>(wb, vo1, vo3, r1, r3);
      }
      asm volatile("s_waitcnt vmcnt(%10)" : "+v"(sr0), "+v"(sr1), "+v"(rb[0]), "+v"(rb[1]), "+v"(rb[2]), "+v"(rb[3]), "+v"(rb[4]), "+v"(rb[5]), "+v"(rb[6]), "+v"(rb[7]) : "n"(MW ? 2 * NE + 2 : 2) : "memory");
      STAMP(1);
      const u32 xb = L_RX + (w - 4) * 2048;
      *LDSP(v4u, xb + lane * 32) = v4u{plo(rb[0], rb[1]), plo(rb[2], rb[3]), plo(rb[4], rb[5]), plo(rb[6], rb[7])};
      *LDSP(v4u, xb + lane * 32 + 16) = v4u{phi(rb[0], rb[1]), phi(rb[2], rb[3]), phi(rb[4], rb[5]), phi(rb[6], rb[7])};
      FENCE();
      v2u pl[4];
#pragma unroll
      for (int i = 0; i < 4; i++) pl[i] = *LDSP(v2u, xb + lane * 8 + i * 512);
      tags();
      const u32 o = WS_XG + b * 14336 + (w - 4) * 4096 + lane * 16;
#pragma unroll
      for (int i = 0; i < 4; i++) st128(v4u{pl[i].x, pl[i].y, t0, t1}, rws, (i < 2 || w < 7) ? o + i * 1024 : OOB);
      FENCE();
      float mr[2], rstd[2];
#pragma unroll
      for (int r = 0; r < 2; r++) {
        const v4u sr = r ? sr1 : sr0;
        const float e[8] = {lo2f(sr.x), hi2f(sr.x), lo2f(sr.y), hi2f(sr.y), lo2f(sr.z), hi2f(sr.z), lo2f(sr.w), hi2f(sr.w)};
        const float s = ((e[0] + e[1]) + (e[2] + e[3])) + ((e[4] + e[5]) + (e[6] + e[7]));
        const float mean = wsum(s) * (1.f / D);
        float q = 0.f;
#pragma unroll
        for (int i = 0; i < 8; i++) { const float d = e[i] - mean; q += d * d; }
        q = wsum(lane < 56 ? q : 0.f);
        rstd[r] = __builtin_amdgcn_rsqf(q * (1.f / D) + 1e-5f);
        mr[r] = -mean * rstd[r];
      }
      waitv<MW ? 2 * NE + 4 : 4>();
      st128(v4u{f2u(lane ? mr[1] : mr[0]), f2u(lane ? rstd[1] : rstd[0]), t0, t1}, rws, lane < 2 ? WS_SG + (u32)(8 * b + 2 * (w - 4) + lane) * 16 : OOB);
      FENCE();
      STAMP(2);
      if constexpr (MW) {
        dmaw2<NE, 14>(wb, vo1, vo3, r1, r3);
        waitv<NE == 14 ? 5 : 0>();
        lsig(L_CNT, lane);
        STAMP(3);
#pragma unroll
        for (int i = 0; i < 4; i++) g[i] = ld128(rws, kvo, i * 4096);
        xg = ld128(rws, xo, 0);
        FENCE();
      } else {
        BAR();
        STAMP(3);
        if (HSLP) __builtin_amdgcn_s_sleep(HSLP);
        const v4u rw2 = mkv(gw2, F * D * 2);
        const int r = pp / 7;
        const u32 o2 = pp < 224 ? (u32)(((r >> 2) * 224 + (ii - 28) * 4 + (r & 3)) * 896 + (pp % 7) * 128) : OOB;
#pragma unroll
        for (int t = 0; t < 14; t++) warm(o2 + t * 14336, rw2);
        WAITV(0);
      }
    };
    if (!mainwg) {
      if (w < 4) kvpart(std::false_type{}); else xpart(std::false_type{});
      STAMP(23);
      STOUT();
      return;
    }
    if (w < 4) kvpart(std::true_type{}); else xpart(std::true_type{});
  }
  PHASE();

  v4u w13[28];
  u32 wra = L_W + (((lane >> 4) & 1) * 4 + 2 * (lane >> 5)) * 1024 + ((lane & 15) >> 2) * 256 +
            (((2 * w + (lane & 1)) ^ (2 * ((lane & 15) >> 2)) ^ (8 * ((lane >> 4) & 1))) * 16) + ((lane >> 1) & 1) * 8;
  OPQ(wra);
  const u32 hdo = WS_HT + (u32)(rt * 57344 + w * 1024 + ((lane >> 2) & 7) * 128 + (lane >> 5) * 64 + (((lane & 3) ^ (2 * (w & 1))) * 16));
  {
    const int la_ = lane & 15, kg = lane >> 4;
    u32 aa[2], ab[2];
#pragma unroll
    for (int s = 0; s < 2; s++) {
      const u32 tb = L_TAP + (ch * 4 + (la_ & 3)) * TAPC, xo = 256 * (la_ >> 3) + 8 * ((la_ >> 2) & 1) + 128 * wq + 64 * s + 16 * kg;
      aa[s] = tb + swz(xo); ab[s] = tb + swz(xo + 8);
      OPQ(aa[s]); OPQ(ab[s]);
    }
    auto lda = [&](int s, int m) { const v2u x = *LDSP(v2u, aa[s] + m * 512), y = *LDSP(v2u, ab[s] + m * 512); return v4u{x.x, x.y, y.x, y.y}; };
    v4u A[2][8], B[2][8];
    lwait(L_CNT);
    STAMP(4);
#pragma unroll
    for (int s = 0; s < 14; s++) w13[s] = trd2<1024>(wra + s * 8192);
#pragma unroll
    for (int m = 0; m < 4; m++) { A[0][m] = lda(0, m); A[1][m] = lda(1, m); }
    lsig(L_CNT + 4, lane);
    {
      const u32 gs = w < 4 ? 64 * 14336 : 4096;
#pragma nounroll
      for (u32 spin = 0; spin < SPIN_MAX; spin++) {
        bool bad[5], any = false;
#pragma unroll
        for (int i = 0; i < 4; i++) { bad[i] = (g[i].z != t0) | (g[i].w != t1); any |= bad[i]; }
        bad[4] = (xg.z != t0) | (xg.w != t1);
        if (!__any(any | bad[4])) break;
        __builtin_amdgcn_s_sleep(PSLP);
        FENCE();
#pragma unroll
        for (int i = 0; i < 4; i++) if (bad[i]) g[i] = ld128(rws, kvo, i * gs);
        if (bad[4]) xg = ld128(rws, xo, 0);
      }
      DRAINV();
    }
    STAMP(5);
    if (w < 4) {
      const u32 la = L_RX + ((tid >> 1) & 1) * RXC + (508 - 8 * (tid >> 2) - 4 * (tid & 1)) * 2;
#pragma unroll
      for (int i = 0; i < 4; i++) *LDSP(v2u, la + (3 - i) * 1024) = v2u{g[i].x, g[i].y};
      if (tid < 32) *LDSP(v4u, L_RX + (tid >> 4) * RXC + 4096 + (tid & 15) * 16) = v4u{0u, 0u, 0u, 0u};
    } else {
      lwait(L_CNT + 4);
#pragma unroll
      for (int i = 0; i < 4; i++) *LDSP(v2u, L_SX0 + (tid - 256) * 8 + i * 2048) = v2u{g[i].x, g[i].y};
      const u32 vo1 = (u32)((4 * (w - 4) + (lane >> 4)) * 3584 + fj * 256) + (((lane & 15) ^ (2 * (lane >> 4))) * 16);
      dmaw2<14, 24>(L_W + (w - 4) * 1024, vo1, vo1 ^ 128, mkv(gw1, D * F * 2), mkv(gw3, D * F * 2));
    }
    u32 so = WS_SG + 16384 + tid * 16;
    u32 ba = L_RX + ch * RXC + (15 + 8 * wq - la_ + kg) * 16;
    u32 ho = (kg & 1) ? OOB : WS_HT + (u32)((tl >> 6) * 57344 + (2 * p + ch) * 128 + (la_ & 7) * 16);
    u32 rdw_ = (w * 3 * 1024 + lane * 16), rdr[3];
#pragma unroll
    for (int q = 1; q < 4; q++) { const int sw = (wq + q) & 3; rdr[q - 1] = ((ch * 4 + sw) * 3 + (wq < sw ? wq : wq - 1)) * 1024; }
    OPQ(so); OPQ(ba); OPQ(ho); OPQ(rdw_);
    const float dval = ch ? hi2f(dv2) : lo2f(dv2);
    auto ldb = [&](int s, int q) { return *LDSP(v4u, ba + s * 64 + (7 - q) * 512); };
    BAR();
    STAMP(6);
    v4u xh, sg0, sg1;
    ald128s(xh, xo + 128 * 14336, rwv); ald128s(sg0, so, rwv); ald128s(sg1, so + 8192, rwv);
#pragma unroll
    for (int q = 0; q < 4; q++) { B[0][q] = ldb(0, q); B[1][q] = ldb(1, q); }
    const v2u kv0 = *LDSP(v2u, L_RX + ch * RXC + (2044 - tl) * 2), kv1 = *LDSP(v2u, L_RX + ch * RXC + (1020 - tl) * 2);
    v4f acc[8];
#pragma unroll
    for (int n = 0; n < 8; n++) acc[n] = v4f{0.f, 0.f, 0.f, 0.f};
    SCHED();
#pragma unroll
    for (int q = 0; q < 4; q++) {
#pragma unroll
      for (int s = 0; s < 2; s++)
#pragma unroll
        for (int m = 0; m + q < 4; m++) acc[m + q] = MFMA16(A[s][m], B[s][q], acc[m + q]);
      if (q == 1) { A[0][7] = lda(0, 7); B[0][7] = ldb(0, 7); }
      if (q == 2) { A[0][6] = lda(0, 6); B[0][6] = ldb(0, 6); }
      SCHED();
    }
    constexpr int PM[26] = {1, 2, 3, 7, 2, 3, 0, 3, 6, 6, 0, 1, 5, 5, 5, 0, 1, 2, 4, 4, 4, 4, 0, 1, 2, 3};
    constexpr int PG[26] = {3, 3, 3, 0, 2, 2, 7, 1, 0, 1, 6, 6, 0, 1, 2, 5, 5, 5, 0, 1, 2, 3, 4, 4, 4, 4};
    auto redw = [&](u32 base, int n0) {
#pragma unroll
      for (int n = 0; n < 4; n++)
        if (n != wq) *LDSP(v4f, base + rdw_ + (n < wq ? n : n - 1) * 1024) = acc[n0 + n];
    };
    auto own = [&](int n0) { v4f y; sfor<0, 4>([&](auto N) { if (N.value == wq) y = acc[n0 + N.value]; }); return y; };
    auto hout = [&](v4f y, v4f sa, v4f sb, v2u kv, v4u x, u32 off) {
      const float x0 = lo2f(x.x), x1 = hi2f(x.x), x2 = lo2f(x.y), x3 = hi2f(x.y);
      const u32 h0 = canon(pk(fmaf(fmaf(x0, sa.y, sa.x), fmaf(hi2f(kv.y), dval, y.x), x0), fmaf(fmaf(x1, sa.w, sa.z), fmaf(lo2f(kv.y), dval, y.y), x1)));
      const u32 h1 = canon(pk(fmaf(fmaf(x2, sb.y, sb.x), fmaf(hi2f(kv.x), dval, y.z), x2), fmaf(fmaf(x3, sb.w, sb.z), fmaf(lo2f(kv.x), dval, y.w), x3)));
      const auto r0 = __builtin_amdgcn_permlane16_swap(h0, h0, false, false), r1 = __builtin_amdgcn_permlane16_swap(h1, h1, false, false);
      st128(v4u{h0, h1, r0[1], r1[1]}, rws, off);
    };
#pragma unroll
    for (int i = 0; i < NPRE; i++) acc[PM[i] + PG[i]] = MFMA16(A[0][PM[i]], B[0][PG[i]], acc[PM[i] + PG[i]]);
    SCHED();
    redw(L_RED0, 0);
    v4f y = own(0);
    STAMP(7);
    BAR();
    STAMP(8);
    {
      v4f pr[3], sa, sb;
#pragma unroll
      for (int q = 0; q < 3; q++) pr[q] = *LDSP(v4f, L_RED0 + rdr[q] + lane * 16);
      sa = *LDSP(v4f, L_SX0 + tl * 8); sb = *LDSP(v4f, L_SX0 + tl * 8 + 16);
      SCHED();
#pragma unroll
      for (int i = NPRE; i < 26; i++) {
        if (i == NPRE) { A[0][5] = lda(0, 5); B[0][5] = ldb(0, 5); }
        if (i == NPRE + 3) { A[0][4] = lda(0, 4); B[0][4] = ldb(0, 4); }
        if (i >= 12 && i < 20) { const int j = 7 - ((i - 12) >> 1); if (i & 1) B[1][j] = ldb(1, j); else A[1][j] = lda(1, j); }
        acc[PM[i] + PG[i]] = MFMA16(A[0][PM[i]], B[0][PG[i]], acc[PM[i] + PG[i]]);
        if (i == NPRE + 2) { y += pr[0] + (pr[1] + pr[2]); }
        if (i == NPRE + 4) { FENCE(); hout(y, sa, sb, kv0, xg, ho); FENCE(); }
        SCHED();
      }
    }
    waitv<4>();
    STAMP(9);
    BAR();
    {
      const v4u rw = mkv(w < 4 ? gw1 : gw3, D * F * 2);
      const u32 vo = (u32)((4 * wq + (lane >> 4)) * 3584 + fj * 256) + (((lane & 15) ^ (2 * (lane >> 4)) ^ (8 * ch)) * 16);
      sfor<0, 26>([&](auto I) {
        constexpr int i = I;
        if constexpr (i < 8 && (i & 1) == 0) dma16<(10 + i / 2) * 8192u, (24 + i / 2) * 57344u>(L_W + w * 1024, vo, rw);
        if constexpr (i == 12) {
          asm volatile("s_waitcnt vmcnt(5)" : "+v"(xh), "+v"(sg0), "+v"(sg1)::"memory");
          fixg(sg0, rws, so, t0, t1); fixg(sg1, rws, so + 8192, t0, t1);
          fixg(xh, rws, xo + 128 * 14336, t0, t1);
        }
        if constexpr (i == 13) *LDSP(v2u, L_SX1 + tid * 8) = v2u{sg0.x, sg0.y};
        if constexpr (i == 15) *LDSP(v2u, L_SX1 + 4096 + tid * 8) = v2u{sg1.x, sg1.y};
        if constexpr (i >= 4 && i < 24 && (i & 1) == 0) w13[12 + i / 2] = trd2<1024>(wra + (i / 2 - 2) * 8192);
        acc[PM[i] + PG[i]] = MFMA16(A[1][PM[i]], B[1][PG[i]], acc[PM[i] + PG[i]]);
        SCHED();
      });
    }
    redw(L_RED1, 4);
    y = own(4);
    WAITV(0);
    STAMP(10);
    BAR();
    STAMP(11);
    sfor<0, 7>([&](auto I) { dma16s<I.value * 8192u, I.value * 8192u>(w * 1024, hdo, rwv); });
    {
      v4f pr[3];
#pragma unroll
      for (int q = 0; q < 3; q++) pr[q] = *LDSP(v4f, L_RED1 + rdr[q] + lane * 16);
      const v4f sa = *LDSP(v4f, L_SX1 + tl * 8), sb = *LDSP(v4f, L_SX1 + tl * 8 + 16);
#pragma unroll
      for (int s = 24; s < 28; s++) w13[s] = trd2<1024>(wra + (s - 14) * 8192);
      y += pr[0] + (pr[1] + pr[2]);
      hout(y, sa, sb, kv1, xh, ho + 16 * 57344);
    }
  }
  FENCE();
  STAMP(12);
  PHASE();

  auto hfix = [&](auto hf_, u32 m) {
    constexpr u32 HF = decltype(hf_)::value;
    if (UNLIKELY(__any(m == 0xFFFFFFFFu))) {
#pragma nounroll
      for (u32 spin = 0; spin < SPIN_MAX; spin++) {
        sfor<0, 7>([&](auto I) { dma16s<HF * L_H + I.value * 8192u, HF * 16 * 57344u + I.value * 8192u>(w * 1024, hdo, rwv); });
        WAITV(0);
        u32 k = 0;
#pragma unroll
        for (int i = 0; i < 7; i++) k = max5(k, *LDSP(v4u, HF * L_H + w * 1024 + lane * 16 + i * 8192));
        if (!__any(k == 0xFFFFFFFFu)) break;
      }
    }
  };
  {
    u32 m = 0;
    sfor<0, 7>([&](auto I) {
      waitv<7 - I.value>();
      m = max5(m, *LDSP(v4u, w * 1024 + lane * 16 + I.value * 8192));
    });
    hfix(IC(0), m);
  }
  const int hh = lane >> 5;
  u32 hfo = WS_HF + (u32)(((rt * 2 + ((lane >> 4) & 1)) * 4 * 56 + 4 * fj + (w >> 1)) * 1024 + ((2 * (w & 1) + hh) * 16 + (lane & 15)) * 16);
  u32 trb = hh * 1024 + ((lane & 15) >> 2) * 64 + ((((lane >> 4) & 1) ^ hh) * 32) + (lane & 3) * 8;
  u32 stb = (u32)((28 * (w & 1) + (lane >> 4)) * 1024 + (w >> 2) * 512 + ((lane & 15) >> 2) * 64 + ((((w >> 1) ^ (lane >> 4)) & 1) * 32) + (lane & 3) * 8);
  u32 w2o = (u32)((224 * w + (lane >> 3)) * 896 + dt * 128 + ((lane & 7) ^ ((lane >> 3) & 2)) * 16);
  u32 w2a = (u32)((lane >> 4) * 1024 + ((lane & 15) >> 2) * 128 + (((lane >> 3) & 1) | ((lane >> 3) & 2)) * 32 + (lane & 3) * 8);
  OPQ(hfo); OPQ(trb); OPQ(stb); OPQ(w2o); OPQ(w2a);
  const u32 cso = w >= 6 ? WS_CS + (u32)((w - 6) * 14336 + fj * 1024 + lane * 16) : OOB;
  const u32 fbo = WS_FB + rt * 128 + (lane < 14 ? lane : 0) * 8;
  const v4u ones = {0x3F803F80u, 0x3F803F80u, 0x3F803F80u, 0x3F803F80u};
  const v4u rw2 = mkv(gw2, F * D * 2);
  const u32 slA = L_SA + w * 4096, slB = w * 4096, slC = w < 6 ? 32768 + w * 4096 : L_SC + (w - 6) * 4096;
  const u32 h2o = WS_HF + (u32)(((rt * 2 + q2) * 4 * 56 + 7 * w) * 1024 + lane * 16);
  const u32 csa = L_CSV + (16 * w + 8 * hh) * 4;
  auto lin = [](int i) -> u32 { return i * 1024u; };
  float nm, ny, cv;
  auto stfin = [&](v4f s) {
    nm = opq(s.x + s.z) * (-1.f / D);
    cv = fmaxf(__builtin_fmaf(-nm, nm, opq(s.y + s.w) * (1.f / D)), 0.f) + 1e-5f;
    ny = -1.4426950408889634f * __builtin_amdgcn_rsqf(cv);
  };
  auto epi = [&](float a, float u, float c1, float c3) -> float {
    const float g_ = opq(__builtin_fmaf(nm, c1, a)), uu = opq(__builtin_fmaf(nm, c3, u));
    const float e = __builtin_amdgcn_exp2f(opq(g_ * ny));
    return opq(opq(g_ * uu) * __builtin_amdgcn_rcpf(opq(__builtin_fmaf(e, cv, cv))));
  };
  v4u wa[7][4];
  auto rdw = [&](int t, u32 sl) {
    const u32 a = w2a + sl;
#pragma unroll
    for (int j = 0; j < 4; j++) wa[t][j] = trd2<512>(a ^ (32u * j));
  };
  auto usew = [&](int t) { asm volatile("" ::"v"(wa[t][0]), "v"(wa[t][1]), "v"(wa[t][2]), "v"(wa[t][3])); };
  auto rdres = [&]() {
    return trd2<256>((u32)((w >> 2) * L_H + (8 * dt + 2 * (lane >> 4) + ((lane >> 1) & 1)) * 1024 + ((w >> 1) & 1) * 512 + ((lane & 15) >> 2) * 64 +
                           ((q2 ^ ((lane >> 1) & 1)) * 32) + 16 * (w & 1) + 8 * (lane & 1)));
  };
  v4u hf[4][7], res, bfr[112], fr[7], csl;
  v2u fbl;
  v16f ac[4];
  v4f sg_, sm_, c1, c3, stv;
  u32 cnt, hm;
  float hv[8];
#define PDX 4
  constexpr int PD = PDX;
  auto ldf = [&](int T) { bfr[T] = trd2<256>(trb + (T / 56) * L_H + ((T / 28) & 1) * 512 + (T % 28) * 2048); };
  BAR();
  STAMP(13);
#pragma unroll
  for (int T = 0; T < PD; T++) ldf(T);
  SCHED();
  sfor<0, 112>([&](auto T_) {
    constexpr int T = T_, nt = T / 28, s = T % 28;
    constexpr int S0 = nt == 0 ? 2 : 19;
    if constexpr (T == 56 - PD) {
      if (w < 4) res = rdres();
      BAR();
      if (w >= 4) res = rdres();
    }
    if constexpr (T + PD < 112) ldf(T + PD);
    if constexpr (find(T_RD, T) >= 0) {
      constexpr int t = find(T_RD, T);
      waitv<nvm(T_W[t][3], T)>();
      rdw(t, t < 4 ? slA : t == 4 ? slB : slC);
    }
    if constexpr (T == T_RD[0]) {
      waitv<nvm(T_CS, T)>(csl);
      if (w >= 6) {
        fixg(csl, rws, cso, t0, t1);
        *LDSP(v2u, L_CSV + (tid - 384) * 8) = v2u{csl.x, csl.y};
      }
      lsig(L_CNT + 8, lane);
    }
    if constexpr (T == T_HV) { waitv<nvm(T_HD[6], T)>(); hm = 0; }
    if constexpr (T >= T_HV && T < T_HV + 7) hm = max5(hm, *LDSP(v4u, L_H + w * 1024 + lane * 16 + (T - T_HV) * 8192));
    if constexpr (T == T_HV + 8) hfix(IC(1), hm);
    if constexpr (T == T_HF[0]) {
      waitv<nvm(T_FB, T)>(fbl);
      if (UNLIKELY(__any((fbl.x != t0) | (fbl.y != t1)))) {
#pragma nounroll
        for (u32 spin = 0; spin < SPIN_MAX; spin++) {
          FENCE();
          fbl = ld64(rws, fbo, 0);
          if (!__any((fbl.x != t0) | (fbl.y != t1))) break;
        }
        DRAINV();
      }
      FENCE();
    }
    if constexpr ((nt & 1) == 0 && s >= S0 && s < S0 + 7) fr[s - S0] = trd2<256>(stb + (nt / 2) * L_H + (s - S0) * 4096);
    if constexpr (nt > 0 && s == 0) {
      if constexpr (nt & 1) { cnt = *LDSP(volatile u32, L_CNT + 8 + (nt / 2) * 4); FENCE(); }
      stv = *LDSP(v4f, L_ST + (32 * (nt - 1) + (lane & 31)) * 16);
      c1 = *LDSP(v4f, csa); c3 = *LDSP(v4f, csa + 512);
    }
    if constexpr (nt > 0 && s == 9) { c1 = *LDSP(v4f, csa + 16); c3 = *LDSP(v4f, csa + 528); }
    if constexpr (s == 0) {
#pragma unroll
      for (int r = 0; r < 16; r++) ac[nt][r] = 0.f;
    }
    ac[nt] = MFMA32(w13[s], bfr[T], ac[nt]);
    if constexpr ((nt & 1) == 0 && s >= S0 + 2 && s < S0 + 9) {
      constexpr int u = s - S0 - 2;
      if constexpr (u == 0) { sg_ = v4f{0.f, 0.f, 0.f, 0.f}; sm_ = sg_; }
      sg_ = MFMA16(fr[u], fr[u], sg_); sm_ = MFMA16(ones, fr[u], sm_);
    }
    if constexpr (T == T_CS) ald128s(csl, cso, rwv);
    if constexpr (T == T_FB) ald64s(fbl, fbo, rwv);
    if constexpr (find(T_PO, T) >= 0) { FENCE(); st128(poison, rws, hfo + find(T_PO, T) * 57344u); FENCE(); }
    if constexpr (T == T_FL) { FENCE(); st64(t0, t1, rws, tid == 0 ? WS_FB + rt * 128 + fj * 8 : OOB); FENCE(); }
    if constexpr (findw(T) >= 0) {
      constexpr int t = findw(T) / 4, i = findw(T) % 4;
      if constexpr (i == 0 && (t == 1 || t == 2 || t == 3)) usew(t - 1);
      if constexpr (i == 0 && t == 6) usew(3);
      dma16<i * 1024u, t * 28672u + i * 7168u>(t == 4 ? slB : t == 5 ? slC : slA, (i & 1) ? w2o ^ 64u : w2o, rw2);
    }
    if constexpr (find(T_HD, T) >= 0) { constexpr u32 i = find(T_HD, T); dma16s<L_H + i * 8192u, 16 * 57344u + i * 8192u>(w * 1024, hdo, rwv); }
    if constexpr (find(T_HF, T) >= 0) { constexpr int i = find(T_HF, T); FENCE(); hf[i / 7][i % 7] = ld128(rws, h2o, ((i / 7) * 56 + i % 7) * 1024); FENCE(); }
    if constexpr (T == (nt == 0 ? 11 : 83) && (nt & 1) == 0) {
      const int i = lane & 3;
      const float sq = i == 0 ? sg_.x : i == 1 ? sg_.y : i == 2 ? sg_.z : sg_.w;
      if (((lane & 15) >> 2) == (lane >> 4)) *LDSP(v2f, L_ST + (64 * (nt / 2) + 16 * (w >> 1) + (lane & 15)) * 16 + (w & 1) * 8) = v2f{sm_.x, sq};
      if constexpr (nt == 2) lsig(L_CNT + 12, lane);
    }
    if constexpr (nt > 0) {
      if constexpr (s == 1) {
        if constexpr (nt & 1)
          if (UNLIKELY(__builtin_amdgcn_readfirstlane(cnt) < 8u)) {
            lwait(L_CNT + 8 + (nt / 2) * 4);
            stv = *LDSP(v4f, L_ST + (32 * (nt - 1) + (lane & 31)) * 16);
            c1 = *LDSP(v4f, csa); c3 = *LDSP(v4f, csa + 512);
          }
        stfin(stv);
      }
      if constexpr (s >= 2 && s < 18 && (s & 1) == 0) { constexpr int r = (s - 2) >> 1; hv[r] = epi(ac[nt - 1][r], ac[nt - 1][r + 8], c1[r & 3], c3[r & 3]); }
      if constexpr (s == 18) { FENCE(); st128(v4u{canon(pk(hv[0], hv[1])), canon(pk(hv[2], hv[3])), canon(pk(hv[4], hv[5])), canon(pk(hv[6], hv[7]))}, rws, hfo); FENCE(); }
      if constexpr (s == 19) { hfo += 56 * 1024; OPQ(hfo); }
    }
    if constexpr (s == 27) { STAMP(14 + nt); }
    SCHED();
  });
  static_assert(T_ST[0] == 46 && T_ST[1] == 74 && T_ST[2] == 102, "H stores are issued at step 18 of a tile");
  {
    stfin(*LDSP(v4f, L_ST + (96 + (lane & 31)) * 16));
#pragma unroll
    for (int h = 0; h < 2; h++) {
      c1 = *LDSP(v4f, csa + 16 * h); c3 = *LDSP(v4f, csa + 512 + 16 * h);
#pragma unroll
      for (int r = 0; r < 4; r++) hv[4 * h + r] = epi(ac[3][4 * h + r], ac[3][4 * h + r + 8], c1[r], c3[r]);
    }
    waitv<nvm(T_W[6][3], 112)>();
    rdw(6, slA);
    FENCE();
    st128(v4u{canon(pk(hv[0], hv[1])), canon(pk(hv[2], hv[3])), canon(pk(hv[4], hv[5])), canon(pk(hv[6], hv[7]))}, rws, hfo);
  }
  FENCE();
  STAMP(18);
  PHASE();
  const u32 pw = (u32)(w * 4096 + lane * 16);
  auto finalize = [&]() {
    const int n = 8 * (w & 1) + (lane & 7);
    const u32 a = (u32)((w >> 1) * 32768 + (lane >> 3) * 512 + (n ^ ((lane >> 1) & 8)) * 16);
    v4f e = *LDSP(v4f, a), o = *LDSP(v4f, a + 256);
#pragma unroll
    for (int u = 1; u < 8; u++) { e += *LDSP(v4f, a + u * 4096); o += *LDSP(v4f, a + u * 4096 + 256); }
    const v4u ov = {pk(lo2f(res.x) + e.x, hi2f(res.x) + e.y), pk(lo2f(res.y) + e.z, hi2f(res.y) + e.w),
                    pk(lo2f(res.z) + o.x, hi2f(res.z) + o.y), pk(lo2f(res.w) + o.z, hi2f(res.w) + o.w)};
    *(v4u*)(gout + ((w >> 2) * 1024 + 64 * rt + 32 * ((w >> 1) & 1) + 16 * q2 + n) * D + 64 * dt + 8 * (lane >> 3)) = ov;
  };
#pragma unroll
  for (int g_ = 0; g_ < 4; g_++) {
    if (g_ == 3) {
      BAR();
      if (w < 6) finalize();
      FENCE();
    }
    settle<7>(hf[g_], rws, h2o + g_ * 56 * 1024, lin);
    STAMP(19 + g_);
    v4f c[4];
#pragma unroll
    for (int i = 0; i < 4; i++) c[i] = v4f{0.f, 0.f, 0.f, 0.f};
#pragma unroll
    for (int s = 0; s < 7; s++)
#pragma unroll
      for (int i = 0; i < 4; i++) c[i] = MFMA16(wa[s][i], hf[g_][s], c[i]);
    SCHED();
    if (g_ == 0) BAR();
    if (g_ < 2) {
#pragma unroll
      for (int s = 0; s < 7; s++) hf[g_ + 2][s] = ld128(rws, h2o, ((g_ + 2) * 56 + s) * 1024);
      FENCE();
    }
#pragma unroll
    for (int i = 0; i < 4; i++) *LDSP(v4f, g_ * 32768 + (pw ^ (128u * (i & 1))) + i * 1024) = c[i];
    SCHED();
  }
  BAR();
  if (w >= 6) finalize();
  if (b == 0 && tid == 0) st64(t0, t1 ^ 0x5bd1e995u, rws, WS_EPOCH);
  STAMP(23);
  STOUT();
}

extern "C" void kernel_launch(void* const* d_in, const int* in_sizes, int n_in, void* d_out, int out_size, void* d_ws, size_t ws_size,
                              hipStream_t stream) {
  (void)hipFuncSetAttribute((const void*)fused_kernel, hipFuncAttributeMaxDynamicSharedMemorySize, L_TOTAL);
  fused_kernel<<<256, 512, L_TOTAL, stream>>>((const u16*)d_in[0], (const u16*)d_in[1], (const u16*)d_in[2], (const u16*)d_in[3],
                                              (const u16*)d_in[4], (const u16*)d_in[5], (const u16*)d_in[7], (const u16*)d_in[8],
                                              (const u16*)d_in[9], (u16*)d_out, (unsigned char*)d_ws);
}
```

```cpp
#include <hip/hip_runtime.h>
#include <hip/hip_bf16.h>
#include <type_traits>

typedef unsigned u32;
typedef unsigned short u16;
typedef unsigned long long u64;
typedef u32 v4u __attribute__((ext_vector_type(4)));
typedef u32 v2u __attribute__((ext_vector_type(2)));
typedef float v4f __attribute__((ext_vector_type(4)));
typedef float v2f __attribute__((ext_vector_type(2)));
typedef float v16f __attribute__((ext_vector_type(16)));
typedef __bf16 bf8 __attribute__((ext_vector_type(8)));
typedef __bf16 bf2 __attribute__((ext_vector_type(2)));
typedef short s4 __attribute__((ext_vector_type(4)));
typedef __amdgpu_buffer_rsrc_t rsrc_t;
#define LDSP(T, off) ((__attribute__((address_space(3))) T*)(size_t)(off))

constexpr int S = 2048, D = 448, F = 1792;
constexpr u32 WS_EPOCH = 0;
constexpr u32 WS_FB = 4096;
constexpr u32 WS_S2 = 16384;
constexpr u32 WS_SG = 49152;
constexpr u32 WS_KVG = 81920;
constexpr u32 WS_XT = WS_KVG + 256u * 224 * 64;
constexpr u32 WS_HT = WS_XT + 256u * 224 * 32;
constexpr u32 WS_HF = WS_HT + 32u * 448 * 128;
constexpr u32 WS_END = WS_HF + 128u * 56 * 1024;
constexpr u32 L_W = 0;
constexpr u32 TAPC = 4416, RXC = 4352;
constexpr u32 L_TAP = 114688;
constexpr u32 L_RX = L_TAP + 8 * TAPC;
constexpr u32 L_RED0 = 81920, L_SX0 = 106496, L_RED1 = 114688, L_SX1 = 139264;
constexpr u32 L_H = 57344;
constexpr u32 L_SA = 114688;
constexpr u32 L_SC = 151552;
constexpr u32 L_X = 147456;
constexpr u32 L_DUM = 159744;
constexpr u32 L_CNT = 163328;
constexpr u32 L_TOTAL = 160 * 1024;
constexpr u32 OOB = 0x80000000u;
constexpr int SC1 = 16;
constexpr u32 SPIN_MAX = 1u << 16;

#define FENCE() asm volatile("" ::: "memory")
#define WAITV(n) asm volatile("s_waitcnt vmcnt(" #n ")" ::: "memory")
#define DRAINV() __builtin_amdgcn_s_waitcnt(0x0F70)
#define BAR() do { asm volatile("s_waitcnt lgkmcnt(0)" ::: "memory"); __builtin_amdgcn_s_barrier(); FENCE(); } while (0)
#define SCHED() __builtin_amdgcn_sched_barrier(0)
#define PHASE() do { SCHED(); asm volatile("" : "+v"(tid)); lane = tid & 63; SCHED(); } while (0)
#define UNLIKELY(x) __builtin_expect(!!(x), 0)
#define USE(x) asm volatile("" ::"v"(x))
#define OPQ(x) asm volatile("" : "+v"(x))
#define IC(n) std::integral_constant<int, n>{}

template <int I, int N, class Fn> __device__ __forceinline__ void sfor(Fn&& f) {
  if constexpr (I < N) { f(std::integral_constant<int, I>{}); sfor<I + 1, N>(f); }
}
template <int N> __device__ __forceinline__ void waitv() { asm volatile("s_waitcnt vmcnt(%0)" ::"n"(N) : "memory"); }
template <int N, class T> __device__ __forceinline__ void waitv(T& a) { asm volatile("s_waitcnt vmcnt(%1)" : "+v"(a) : "n"(N) : "memory"); }
__device__ __forceinline__ rsrc_t mk(const void* p, u32 bytes) { return __builtin_amdgcn_make_buffer_rsrc((void*)p, 0, bytes, 0x00020000); }
__device__ __forceinline__ v4u mkv(const void* p, u32 bytes) { const u64 a = (u64)p; return v4u{(u32)a, (u32)(a >> 32) & 0xffffu, bytes, 0x00020000u}; }
__device__ __forceinline__ float lo2f(u32 v) { return __builtin_bit_cast(float, v << 16); }
__device__ __forceinline__ float hi2f(u32 v) { return __builtin_bit_cast(float, v & 0xffff0000u); }
__device__ __forceinline__ u32 pk(float a, float b) { return __builtin_bit_cast(u32, __builtin_convertvector(v2f{a, b}, bf2)); }
__device__ __forceinline__ u32 plo(u32 a, u32 b) { return __builtin_amdgcn_perm(b, a, 0x05040100u); }
__device__ __forceinline__ u32 phi(u32 a, u32 b) { return __builtin_amdgcn_perm(b, a, 0x07060302u); }
__device__ __forceinline__ u32 canon(u32 v) { return min(v, 0xFFFFFFFEu); }
__device__ __forceinline__ u32 swz(u32 a) { return a ^ ((a >> 3) & 32u); }
__device__ __forceinline__ bf8 asbf(v4u v) { return __builtin_bit_cast(bf8, v); }
__device__ __forceinline__ v2u trd(u32 a) { return __builtin_bit_cast(v2u, __builtin_amdgcn_ds_read_tr16_b64_v4i16(LDSP(s4, a))); }
template <int O> __device__ __forceinline__ v4u trd2(u32 a) { const v2u x = trd(a), y = trd(a + O); return v4u{x.x, x.y, y.x, y.y}; }
__device__ __forceinline__ void st128(v4u v, rsrc_t r, u32 off) { __builtin_amdgcn_raw_buffer_store_b128(v, r, off, 0, SC1); }
__device__ __forceinline__ void st64(u32 a, u32 b_, rsrc_t r, u32 off) { __builtin_amdgcn_raw_buffer_store_b64(v2u{a, b_}, r, off, 0, SC1); }
__device__ __forceinline__ v4u ld128(rsrc_t r, u32 off, u32 so) { return __builtin_amdgcn_raw_buffer_load_b128(r, off, so, SC1); }
__device__ __forceinline__ v2u ld64(rsrc_t r, u32 off, u32 so) { return __builtin_amdgcn_raw_buffer_load_b64(r, off, so, SC1); }
__device__ __forceinline__ u32 f2u(float v) { return __builtin_bit_cast(u32, v); }
__device__ __forceinline__ float u2f(u32 v) { return __builtin_bit_cast(float, v); }
__device__ __forceinline__ float opq(float v) { asm("" : "+v"(v)); return v; }
template <int CTRL> __device__ __forceinline__ float dppadd(float v) { return v + u2f(__builtin_amdgcn_update_dpp(0, f2u(v), CTRL, 0xF, 0xF, true)); }
__device__ __forceinline__ float swap16add(float a, float b) { const auto r = __builtin_amdgcn_permlane16_swap(f2u(a), f2u(b), false, false); return u2f(r[0]) + u2f(r[1]); }
__device__ __forceinline__ float swap32add(float a, float b) { const auto r = __builtin_amdgcn_permlane32_swap(f2u(a), f2u(b), false, false); return u2f(r[0]) + u2f(r[1]); }
__device__ __forceinline__ float wsum(float v) {
  v = dppadd<0xB1>(v); v = dppadd<0x4E>(v); v = dppadd<0x141>(v); v = dppadd<0x140>(v);
  v = swap16add(v, v);
  return swap32add(v, v);
}
template <int OFF> __device__ __forceinline__ void ald32(u32& d, u32 vo, v4u r, u32 so) {
  asm volatile("buffer_load_dword %0, %1, %2, %3 offen offset:%4" : "=v"(d) : "v"(vo), "s"(r), "s"(so), "n"(OFF) : "memory");
}
__device__ __forceinline__ void ald32s(u32& d, u32 vo, v4u r, u32 so) {
  asm volatile("buffer_load_dword %0, %1, %2, %3 offen sc1" : "=v"(d) : "v"(vo), "s"(r), "s"(so) : "memory");
}
template <int OFF> __device__ __forceinline__ void ald128(v4u& d, u32 vo, v4u r) {
  asm volatile("buffer_load_dwordx4 %0, %1, %2, 0 offen offset:%3" : "=v"(d) : "v"(vo), "s"(r), "n"(OFF) : "memory");
}
__device__ __forceinline__ void ald128s(v4u& d, u32 vo, v4u r) { asm volatile("buffer_load_dwordx4 %0, %1, %2, 0 offen sc1" : "=v"(d) : "v"(vo), "s"(r) : "memory"); }
template <bool DEV> __device__ __forceinline__ void ald128so(v4u& d, u32 vo, v4u r, u32 so) {
  if constexpr (DEV) asm volatile("buffer_load_dwordx4 %0, %1, %2, %3 offen sc1" : "=v"(d) : "v"(vo), "s"(r), "s"(so) : "memory");
  else asm volatile("buffer_load_dwordx4 %0, %1, %2, %3 offen" : "=v"(d) : "v"(vo), "s"(r), "s"(so) : "memory");
}
__device__ __forceinline__ void ald64s(v2u& d, u32 vo, v4u r) { asm volatile("buffer_load_dwordx2 %0, %1, %2, 0 offen sc1" : "=v"(d) : "v"(vo), "s"(r) : "memory"); }
template <u32 LI, u32 SO> __device__ __forceinline__ void dma16(u32 lb, u32 vo, v4u r) {
  u32 t;
  asm volatile("s_add_u32 m0, %1, %4\n\ts_mov_b32 %0, %5\n\tbuffer_load_dwordx4 %2, %3, %0 offen lds"
               : "=&s"(t) : "s"(lb), "v"(vo), "s"(r), "n"(LI), "n"(SO) : "memory", "scc");
}
template <u32 LI, u32 SO, bool DEV = true> __device__ __forceinline__ void dma16s(u32 lb, u32 vo, v4u r) {
  if constexpr (!DEV) { dma16<LI, SO>(lb, vo, r); return; }
  u32 t;
  asm volatile("s_add_u32 m0, %1, %4\n\ts_mov_b32 %0, %5\n\tbuffer_load_dwordx4 %2, %3, %0 offen sc1 lds"
               : "=&s"(t) : "s"(lb), "v"(vo), "s"(r), "n"(LI), "n"(SO) : "memory", "scc");
}
template <u32 LI, u32 SO> __device__ __forceinline__ void dma16n(u32 lb, u32 vo, v4u r) {
  u32 t;
  asm volatile("s_add_u32 m0, %1, %4\n\ts_mov_b32 %0, %5\n\tbuffer_load_dwordx4 %2, %3, %0 offen lds"
               : "=&s"(t) : "s"(lb), "v"(vo), "s"(r), "n"(LI), "n"(SO) : "memory", "scc");
}
__device__ __forceinline__ void warm(u32 vo, v4u r) {
  asm volatile("s_mov_b32 m0, %2\n\ts_nop 0\n\tbuffer_load_dword %0, %1, 0 offen lds" ::"v"(vo), "s"(r), "n"(L_DUM) : "memory");
}
template <u32 A> __device__ __forceinline__ void lsig() {
  u32 t, o;
  asm volatile("v_mov_b32 %0, %2\n\tv_mov_b32 %1, 1\n\ts_mov_b64 exec, 1\n\tds_add_u32 %0, %1\n\ts_mov_b64 exec, -1" : "=&v"(t), "=&v"(o) : "n"(A) : "memory");
}
template <u32 A> __device__ __forceinline__ void lsigr(u32& o) {
  u32 t;
  asm volatile("v_mov_b32 %0, %2\n\tv_mov_b32 %1, 1\n\ts_mov_b64 exec, 1\n\tds_add_rtn_u32 %1, %0, %1\n\ts_mov_b64 exec, -1" : "=&v"(t), "=&v"(o) : "n"(A) : "memory");
}
__device__ __forceinline__ void lwait(u32 a, u32 n = 8u) {
#pragma nounroll
  for (u32 spin = 0; spin < SPIN_MAX * 16; spin++) {
    FENCE();
    if (__builtin_amdgcn_readfirstlane(*LDSP(volatile u32, a)) >= n) break;
    __builtin_amdgcn_s_sleep(1);
  }
  FENCE();
}
#define MFMA32(a, b, c) __builtin_amdgcn_mfma_f32_32x32x16_bf16(asbf(a), asbf(b), c, 0, 0, 0)
#define MFMA16(a, b, c) __builtin_amdgcn_mfma_f32_16x16x32_bf16(asbf(a), asbf(b), c, 0, 0, 0)

constexpr int NR2A = 4, XAT = 9, R3A = 13, PWA = 20, PWB = 46, W13A = 160, HD0 = 14, HV0 = 44, TG0 = 27, TG1 = 70, EP0 = 6, NW0 = 3, PZ0 = 2, PZD = 2, FAST2 = 1, PRI0 = 78, HF2U = 10, HF2D = 3, HF3U = 62, HF3D = 2, PDX = 4;

template <int N, class SOF> __device__ __forceinline__ bool settle(v4u* t, rsrc_t r, u32 off, SOF so) {
  u32 m = 0;
#pragma unroll
  for (int i = 0; i < N; i++) m = max(m, t[i].x);
  if (UNLIKELY(__any(m == 0xFFFFFFFFu))) {
#pragma nounroll
    for (u32 spin = 0; spin < SPIN_MAX; spin++) {
      bool any = false;
      FENCE();
#pragma unroll
      for (int i = 0; i < N; i++)
        if (__any(t[i].x == 0xFFFFFFFFu)) { t[i] = ld128(r, off, so(i)); any = true; }
      if (!any) break;
    }
    DRAINV();
    return true;
  }
  return false;
}
__device__ __forceinline__ bool fixg(v4u& g, rsrc_t r, u32 off, u32 t0, u32 t1) {
  if (UNLIKELY(__any((g.z != t0) | (g.w != t1)))) {
#pragma nounroll
    for (u32 spin = 0; spin < SPIN_MAX; spin++) {
      FENCE();
      g = ld128(r, off, 0);
      if (!__any((g.z != t0) | (g.w != t1))) break;
    }
    DRAINV();
    return true;
  }
  return false;
}

constexpr int T_FS = 28 + EP0, T_FL = T_FS + 6, T_FB = 75;
constexpr int T_W[7][4] = {{1, 3, 5, 7}, {21, 22, 23, 24}, {37, 39, 41, 43}, {57, 59, 61, 63}, {65, 67, 69, 71}, {73, 77, 79, 81}, {93, 95, 97, 99}};
constexpr int T_RD[6] = {20, 36, 55, 90, 99, 103};
constexpr int T_HD[7] = {HD0, HD0 + 1, HD0 + 2, HD0 + 3, HD0 + 4, HD0 + 5, HD0 + 6};
constexpr int T_HV = HV0;
constexpr int T_PZ[4] = {PZ0, PZ0 + PZD, PZ0 + 2 * PZD, PZ0 + 3 * PZD};
constexpr int T_G[2] = {TG0, TG1};
constexpr int T_EP[3] = {28 + EP0, 56, 84};
constexpr int T_ST[3] = {T_EP[0] + 20, T_EP[1] + 20, T_EP[2] + (FAST2 ? 14 : 20)};
constexpr int T_HF[14] = {85, 86, 87, 92, 94, 96, FAST2 ? 104 : 98, 105, 106, 107, 108, 109, 110, 111};
constexpr int nvm(int a, int b) {
  int n = 0;
  auto in = [&](int t) { if (t > a && t < b) n++; };
  in(T_FB); in(T_FL); in(T_G[0]); in(T_G[1]);
  for (int i = 0; i < 7; i++) { in(T_HD[i]);
    for (int j = 0; j < 4; j++) in(T_W[i][j]);
  }
  for (int i = 0; i < 3; i++) in(T_ST[i]);
  for (int i = 0; i < 4; i++) in(T_PZ[i]);
  for (int i = 0; i < 14; i++) in(T_HF[i]);
  return n;
}
constexpr bool distinct() { for (int t = 0; t < 112; t++) if (nvm(t - 1, t + 1) > 1) return false; return true; }
static_assert(distinct(), "one memory operation per step");
template <int N> constexpr int find(const int (&a)[N], int t) { for (int i = 0; i < N; i++) if (a[i] == t) return i; return -1; }
constexpr int findw(int t) { for (int i = 0; i < 28; i++) if (T_W[i / 4][i % 4] == t) return i; return -1; }

constexpr u32 wslot(u32 i) { return i < 24 ? (i < 10 ? i : i - 10) * 8192u : i == 24 ? L_X : (i - 18) * 8192u; }

__global__ void __launch_bounds__(512, 2)
fused_kernel(const u16* __restrict__ gx, const u16* __restrict__ gk, const u16* __restrict__ gv, const u16* __restrict__ gsk,
             const u16* __restrict__ gssm, const u16* __restrict__ gsd, const u16* __restrict__ gw1, const u16* __restrict__ gw3,
             const u16* __restrict__ gw2, u16* __restrict__ gout, unsigned char* __restrict__ ws) {
  asm volatile("" ::"s"(gx), "s"(gk), "s"(gv), "s"(gsk), "s"(gssm), "s"(gsd), "s"(gw1), "s"(gw3), "s"(gw2), "s"(gout), "s"(ws));
  int tid = threadIdx.x, lane = tid & 63;
  const int w = __builtin_amdgcn_readfirstlane(tid >> 6), b = blockIdx.x;
  const int xc = b & 7, ii = b >> 3;
  const bool mainwg = ii < 28;
  const int p = xc * 28 + ii;
  const int dt = ii % 7, q2 = xc & 1;
  const int rt = 4 * (xc >> 1) + ii / 7, fj = 7 * q2 + dt;
  const rsrc_t rws = mk(ws, WS_END);
  const v4u rwv = mkv(ws, WS_END);
  const v4u poison = {0xFFFFFFFFu, 0xFFFFFFFFu, 0xFFFFFFFFu, 0xFFFFFFFFu};
  const v2u ep = *(const __attribute__((address_space(4))) v2u*)(ws + WS_EPOCH);
  const u32 dv2 = *(const __attribute__((address_space(4))) u32*)(gsd + 2 * (mainwg ? p : 0));
  u32 t0, t1;
  auto tags = [&]() { t0 = ep.x + 1u; t1 = ep.y ^ 0x5bd1e995u; asm volatile("" : "+s"(t0), "+s"(t1)); };

  const int ch = w >> 2, wq = w & 3;
  const int tl = 256 * wq + 128 * (lane >> 5) + 8 * (lane & 15) + 4 * ((lane >> 4) & 1);
  const v4u rxv = mkv(gx, S * D * 2);
  {
    const int pp = tid & 255;
    auto kvpart = [&](auto mw) {
      constexpr bool MW = decltype(mw)::value;
      const u32 colo = pp < 224 ? (u32)(8 * b * 896 + pp * 4) : OOB;
      u32 ra[11], rb[8];
      const rsrc_t rk = mk(gk, S * D * 2), rv = mk(gv, S * D * 2);
      const u32 halo = (b > 0 && pp < 224) ? colo - 3 * 896 : OOB;
#pragma unroll
      for (int i = 0; i < 3; i++) ra[i] = __builtin_amdgcn_raw_buffer_load_b32(rk, halo + i * 896, 0, 0);
#pragma unroll
      for (int i = 0; i < 8; i++) ra[3 + i] = __builtin_amdgcn_raw_buffer_load_b32(rk, colo + (i & 3) * 896, (i >> 2) * 3584, 0);
      const v4u skw = __builtin_amdgcn_raw_buffer_load_b128(mk(gsk, D * 8), pp * 16, 0, 0);
#pragma unroll
      for (int i = 0; i < 8; i++) rb[i] = __builtin_amdgcn_raw_buffer_load_b32(rv, colo + (i & 3) * 896, (i >> 2) * 3584, 0);
      FENCE();
      v4u tp[4];
      auto ldtaps = [&]() {
        const rsrc_t rt_ = mk(gssm, D * S * 2);
        const u32 o = (u32)(2 * p * 4096 + pp * 16);
#pragma unroll
        for (int c = 0; c < 2; c++) {
          tp[2 * c] = __builtin_amdgcn_raw_buffer_load_b128(rt_, pp > 0 ? o - 16 : OOB, c * 4096, 0);
          tp[2 * c + 1] = __builtin_amdgcn_raw_buffer_load_b128(rt_, o, c * 4096, 0);
        }
        FENCE();
      };
      if constexpr (MW) {
        ldtaps();
        if (tid < 2) *LDSP(v4u, L_CNT + tid * 16) = v4u{0u, 0u, 0u, 0u};
      }
      const u32 o = WS_KVG + b * 14336 + w * 4096 + lane * 16, o2 = w < 3 ? o + 2048 : OOB;
      USE(rb[7]);
      const float s0[4] = {lo2f(skw.x), hi2f(skw.x), lo2f(skw.y), hi2f(skw.y)};
      const float s1[4] = {lo2f(skw.z), hi2f(skw.z), lo2f(skw.w), hi2f(skw.w)};
      float k0[8], k1[8];
#pragma unroll
      for (int i = 0; i < 8; i++) {
        float a0 = lo2f(ra[i + 3]) * s0[0], a1 = hi2f(ra[i + 3]) * s1[0];
#pragma unroll
        for (int j = 1; j < 4; j++) { a0 += lo2f(ra[i + 3 - j]) * s0[j]; a1 += hi2f(ra[i + 3 - j]) * s1[j]; }
        k0[i] = a0 * lo2f(rb[i]); k1[i] = a1 * hi2f(rb[i]);
      }
      const u32 sc = L_TAP + 256 + w * 1024;
      *LDSP(v4u, sc + (lane >> 5) * TAPC + (lane & 31) * 32) = v4u{pk(k0[3], k0[2]), pk(k0[1], k0[0]), pk(k0[7], k0[6]), pk(k0[5], k0[4])};
      *LDSP(v4u, sc + (lane >> 5) * TAPC + (lane & 31) * 32 + 16) = v4u{pk(k1[3], k1[2]), pk(k1[1], k1[0]), pk(k1[7], k1[6]), pk(k1[5], k1[4])};
      tags();
      FENCE();
      v2u pl[4];
#pragma unroll
      for (int i = 0; i < 4; i++) pl[i] = *LDSP(v2u, sc + lane * 8 + (i >> 1) * TAPC + (i & 1) * 512);
#pragma unroll
      for (int i = 0; i < 4; i++) st128(v4u{pl[i].x, pl[i].y, t0, t1}, rws, (i < 2 ? o : o2) + (i & 1) * 1024);
      FENCE();
      SCHED();
      BAR();
      SCHED();
      if constexpr (MW) {
#pragma unroll
        for (int i = 0; i < 4; i++) OPQ(tp[i]);
#pragma unroll
        for (int c = 0; c < 2; c++) {
          const u32 a[8] = {tp[2 * c].x, tp[2 * c].y, tp[2 * c].z, tp[2 * c].w, tp[2 * c + 1].x, tp[2 * c + 1].y, tp[2 * c + 1].z, tp[2 * c + 1].w};
          u32 od[7];
#pragma unroll
          for (int j = 0; j < 7; j++) od[j] = __builtin_amdgcn_alignbit(a[j + 1], a[j], 16);
          const u32 base = L_TAP + c * 4 * TAPC + swz((pp + 16) * 16);
          *LDSP(v4u, base) = v4u{od[0], od[1], od[2], od[3]};
          *LDSP(v4u, base + TAPC) = v4u{a[1], a[2], a[3], a[4]};
          *LDSP(v4u, base + 2 * TAPC) = v4u{od[1], od[2], od[3], od[4]};
          *LDSP(v4u, base + 3 * TAPC) = v4u{a[2], a[3], a[4], a[5]};
          if (pp == 255) {
            const u32 e = L_TAP + c * 4 * TAPC + 4384;
            *LDSP(v2u, e) = v2u{od[4], od[5]};
            *LDSP(v2u, e + TAPC) = v2u{a[5], a[6]};
            *LDSP(v2u, e + 2 * TAPC) = v2u{od[5], od[6]};
            *LDSP(v2u, e + 3 * TAPC) = v2u{a[6], a[7]};
          }
        }
        if (tid < 128) *LDSP(v4u, L_TAP + (tid >> 4) * TAPC + (tid & 15) * 16) = v4u{0u, 0u, 0u, 0u};
        if (tid < 32) *LDSP(v4u, L_RX + (tid >> 4) * RXC + 4096 + (tid & 15) * 16) = v4u{0u, 0u, 0u, 0u};
        lsig<L_CNT>();
      }
    };
    auto xpart = [&]() {
      const int c = lane >> 2;
      const u32 so = (u32)((8 * b + 4 * (w - 4) + (lane & 3)) * 896 + c * 16);
      v4u sr[4];
      ald128<0>(sr[0], so, rxv); ald128<256>(sr[1], so, rxv); ald128<512>(sr[2], so, rxv); ald128<768>(sr[3], c < 8 ? so : OOB, rxv);
      const u32 to = (u32)((8 * b + 4 * (lane & 1)) * 896 + (w - 4) * 512 + (lane >> 1) * 4), to3 = w == 4 ? to : OOB;
      u32 xt[4][4];
      sfor<0, 16>([&](auto I) { constexpr int m = I.value >> 2, r = I.value & 3; ald32<m * 128 + r * 896>(xt[m][r], m < 3 ? to : to3, rxv, 0); });
      auto hpz = [&]() {
        const u32 o = WS_HT + (u32)(b * 7168 + (w - 4) * 3584 + lane * 16);
#pragma unroll
        for (int i = 0; i < 4; i++) st128(poison, rws, (i < 3 || lane < 32) ? o + i * 1024 : OOB + i * 1024);
        FENCE();
      };
      hpz();
      asm volatile("s_waitcnt vmcnt(%4)" : "+v"(sr[0]), "+v"(sr[1]), "+v"(sr[2]), "+v"(sr[3]) : "n"(4) : "memory");
#pragma unroll
      for (int m = 0; m < 4; m++) asm volatile("" : "+v"(xt[m][0]), "+v"(xt[m][1]), "+v"(xt[m][2]), "+v"(xt[m][3]));
      auto xst = [&]() {
        const u32 o = WS_XT + (u32)(b * 7168 + (w - 4) * 4096 + lane * 16);
#pragma unroll
        for (int m = 0; m < 4; m++) st128(v4u{xt[m][0], xt[m][1], xt[m][2], xt[m][3]}, rws, (m < 3 || w == 4) ? o + m * 1024 : OOB + m * 1024);
        FENCE();
      };
      xst();
      tags();
      auto rsum = [](float v) { v = dppadd<0x124>(v); v = dppadd<0x128>(v); v = swap16add(v, v); return swap32add(v, v); };
      float e[4][8], sn[4];
#pragma unroll
      for (int n = 0; n < 4; n++) {
#pragma unroll
        for (int j = 0; j < 4; j++) { const u32 v = sr[n][j]; e[n][2 * j] = lo2f(v); e[n][2 * j + 1] = hi2f(v); }
        sn[n] = ((e[n][0] + e[n][1]) + (e[n][2] + e[n][3])) + ((e[n][4] + e[n][5]) + (e[n][6] + e[n][7]));
      }
      const float mean = rsum((sn[0] + sn[1]) + (sn[2] + sn[3])) * (1.f / D);
#pragma unroll
      for (int n = 0; n < 4; n++) {
        sn[n] = 0.f;
#pragma unroll
        for (int i = 0; i < 8; i++) { e[n][i] -= mean; sn[n] += e[n][i] * e[n][i]; }
      }
      const float rs = __builtin_amdgcn_rsqf(rsum((sn[0] + sn[1]) + (sn[2] + (c < 8 ? sn[3] : 0.f))) * (1.f / D) + 1e-5f);
      __builtin_amdgcn_s_barrier();
      WAITV(0);
      st128(v4u{f2u(-mean * rs), f2u(rs), t0, t1}, rws, lane < 4 ? WS_SG + (u32)(8 * b + 4 * (w - 4) + lane) * 16 : OOB);
      FENCE();
    };
    if (!mainwg) {
      if (w < 4) kvpart(std::false_type{}); else if (w < 6) xpart(); else { tags(); __builtin_amdgcn_s_barrier(); }
      const int j = ii - 28;
      const v4u rw13 = mkv((j & 1) ? gw3 : gw1, D * F * 2), rw2 = mkv(gw2, F * D * 2);
      const u32 o1 = tid < 448 ? (u32)((W13A + (j >> 1) * 32 + tid / 14) * 3584 + q2 * 1792 + (tid % 14) * 128) : OOB;
      const int r = tid / 7;
      const u32 o2 = tid < 448 ? (u32)(((r >> 3) * 224 + 8 * j + (r & 7)) * 896 + (tid % 7) * 128) : OOB;
      constexpr int N13 = (448 - W13A + 63) / 64;
      const int gj = xc * 4 + j;
      warm(tid < 224 ? (u32)(((gj >> 2) * 224 + 32 * (NW0 + (gj & 3)) + tid / 7) * 896 + (tid % 7) * 128) : OOB, rw2);
      __builtin_amdgcn_s_barrier();
#pragma unroll
      for (int t = 0; t < N13; t++) warm(o1 + (t + (xc >> 1)) % N13 * 64 * 3584, rw13);
      __builtin_amdgcn_s_barrier();
#pragma unroll
      for (int t = 0; t < NW0; t++) {
        warm(o2 + (t + xc) % NW0 * 28672, rw2);
        __builtin_amdgcn_s_barrier();
      }
#pragma unroll
      for (int u = 0; u < 2; u++) {
        const u32 hf_ = 16 * u + 4 * (xc >> 1) + j;
        const u32 go = WS_SG + (64 * hf_ + lane) * 16, ho = WS_HT + hf_ * 57344 + (56 * w + (lane >> 2)) * 128 + q2 * 64 + (lane & 3) * 16;
        v4u g = ld128(rws, go, 0), t[4];
        fixg(g, rws, go, t0, t1);
        FENCE();
#pragma unroll
        for (int i = 0; i < 3; i++) t[i] = ld128(rws, ho, i * 2048);
        t[3] = ld128(rws, lane < 32 ? ho : OOB, 3 * 2048);
        FENCE();
        settle<3>(t, rws, ho, [](int i) -> u32 { return i * 2048u; });
        settle<1>(t + 3, rws, lane < 32 ? ho : OOB, [](int i) -> u32 { return 3 * 2048u; });
        float z[8];
#pragma unroll
        for (int e = 0; e < 8; e++) {
          float sm = 0.f, sq = 0.f;
#pragma unroll
          for (int i = 0; i < 4; i++) { const u32 v = t[i][e >> 1]; const float x = (e & 1) ? hi2f(v) : lo2f(v); sm += x; sq = __builtin_fmaf(x, x, sq); }
          z[e] = swap16add(sm, sq);
        }
        v4f q;
#pragma unroll
        for (int e = 0; e < 4; e++) q[e] = dppadd<0x128>(dppadd<0x124>(swap32add(z[e], z[e + 4])));
        if ((lane & 15) < 4) *LDSP(v4f, u * 2048 + w * 256 + ((lane >> 4) & 1) * 128 + (lane & 3) * 32 + (lane >> 5) * 16) = q;
        BAR();
        if (w == 0) {
          float a = 0.f;
#pragma unroll
          for (int i = 0; i < 8; i++) a += *LDSP(float, u * 2048 + i * 256 + lane * 4);
          const auto r = __builtin_amdgcn_permlane32_swap(f2u(a), f2u(a), false, false);
          const float nm = u2f(r[0]) * (-1.f / D), cv = fmaxf(__builtin_fmaf(-nm, nm, u2f(r[1]) * (1.f / D)), 0.f) + 1e-5f;
          st128(v4u{pk(nm, nm - lo2f(pk(nm, nm))), f2u(cv), t0, t1}, rws, lane < 32 ? WS_S2 + (64 * hf_ + 32 * q2 + lane) * 16 : OOB);
        }
        FENCE();
        __builtin_amdgcn_s_barrier();
        FENCE();
        if (u == 1) {
#pragma unroll
          for (int t = NW0; t < 7; t++) warm(o2 + t * 28672, rw2);
        }
      }
      WAITV(0);
      return;
    }
    if (w < 4) {
      kvpart(std::true_type{});
    } else if (w < 6) {
      xpart();
      lsig<L_CNT>();
    } else {
      const v4u rw = mkv(w == 6 ? gw1 : gw3, D * F * 2);
      const u32 vo = (u32)((lane >> 4) * 3584 + fj * 256) + (((lane & 15) ^ (2 * (lane >> 4)) ^ (8 * (w & 1))) * 16);
      u32 st = (u32)(rt * 5) >> 3;
      auto wld = [&](int n) {
#pragma nounroll
        for (int i = 0; i < n; i++) {
          const u32 lb = L_W + st * 8192 + (w & 1) * 4096, vs = vo + st * 57344;
          dma16n<0, 0>(lb, vs, rw); dma16n<1024, 14336>(lb, vs, rw); dma16n<2048, 28672>(lb, vs, rw); dma16n<3072, 43008>(lb, vs, rw);
          st = st == 9 ? 0 : st + 1;
        }
      };
      tags();
      __builtin_amdgcn_s_barrier();
      wld(10);
      WAITV(0);
      lsig<L_CNT>();
    }
  }
  PHASE();

  v4u w13[28];
  u32 wra = L_W + (((lane >> 4) & 1) * 4 + 2 * (lane >> 5)) * 1024 + ((lane & 15) >> 2) * 256 +
            (((2 * w + (lane & 1)) ^ (2 * ((lane & 15) >> 2)) ^ (8 * ((lane >> 4) & 1))) * 16) + ((lane >> 1) & 1) * 8;
  OPQ(wra);
  const u32 hdo = WS_HT + (u32)(rt * 57344 + w * 1024 + ((lane >> 2) & 7) * 128 + (lane >> 5) * 64 + (((lane & 3) ^ (2 * (w & 1))) * 16));
  u32 hfo = WS_HF + (u32)(((rt * 2 + ((lane >> 4) & 1)) * 4 * 56 + 4 * fj + (w >> 1)) * 1024 + ((2 * (w & 1) + (lane >> 5)) * 16 + (lane & 15)) * 16);
  OPQ(hfo);
  v4f cs;
  float cs0;
  auto colsum = [&](int s0, int s1) {
    u32 o = ((lane >> 2) & 3) == ((lane >> 4) & 1) ? 0x3F803F80u : 0u;
    OPQ(o);
    const v4u ind = {o, o, o, o};
#pragma unroll
    for (int s = s0; s < s1; s++) cs = MFMA16(ind, w13[s], cs);
  };
  {
    const int la_ = lane & 15, kg = lane >> 4;
    u32 aa[2], ab[2];
#pragma unroll
    for (int s = 0; s < 2; s++) {
      const u32 tb = L_TAP + (ch * 4 + (la_ & 3)) * TAPC, xo = 256 * (la_ >> 3) + 8 * ((la_ >> 2) & 1) + 128 * wq + 64 * s + 16 * kg;
      aa[s] = tb + swz(xo); ab[s] = tb + swz(xo + 8);
      OPQ(aa[s]); OPQ(ab[s]);
    }
    auto lda = [&](int s, int m) { const v2u x = *LDSP(v2u, aa[s] + m * 512), y = *LDSP(v2u, ab[s] + m * 512); return v4u{x.x, x.y, y.x, y.y}; };
    v4u A[2][8], B[2][8];
    v4u g[4];
    u32 x0[4] = {};
    u32 kvo = w < 4 ? WS_KVG + (tid >> 2) * 14336 + p * 64 + (tid & 3) * 16 : WS_SG + (tid - 256) * 16;
    u32 so = WS_SG + 16384 + tid * 16, xo = WS_XT + (u32)((tid >> 3) * 7168 + p * 32 + (tid & 7) * 4);
    u32 ba = L_RX + ch * RXC + (15 + 8 * wq - la_ + kg) * 16;
    u32 ho = (kg & 1) ? OOB : WS_HT + (u32)((tl >> 6) * 57344 + (2 * p + ch) * 128 + (la_ & 7) * 16);
    u32 rdw_ = (w * 3 * 1024 + lane * 16), rdr[3];
    u32 vo = (u32)((4 * wq + kg) * 3584 + fj * 256) + ((la_ ^ (2 * kg) ^ (8 * ch)) * 16);
#pragma unroll
    for (int q = 1; q < 4; q++) { const int sw = (wq + q) & 3; rdr[q - 1] = ((ch * 4 + sw) * 3 + (wq < sw ? wq : wq - 1)) * 1024; }
    OPQ(kvo); OPQ(so); OPQ(xo); OPQ(ba); OPQ(ho); OPQ(rdw_); OPQ(vo);
    const u32 rq = (u32)ii;
    if (w < 4) {
      WAITV(0);
#pragma unroll
      for (int i = 0; i < 4; i++) g[i] = ld128(rws, kvo, i * 64 * 14336);
      FENCE();
    }
    lwait(L_CNT);
    if (w >= 4) {
#pragma unroll
      for (int i = 0; i < 4; i++) g[i] = __builtin_amdgcn_raw_buffer_load_b128(rws, kvo, ((i + rq) & 3) * 4096, SC1);
    }
    FENCE();
#pragma unroll
    for (int s = 0; s < 10; s++) w13[s] = trd2<1024>(wra + s * 8192);
#pragma unroll
    for (int m = 0; m < 4; m++) { A[0][m] = lda(0, m); A[1][m] = lda(1, m); }
    auto poll4 = [&](auto gs) {
#pragma nounroll
      for (u32 spin = 0; spin < SPIN_MAX; spin++) {
        bool bad[4], any = false;
#pragma unroll
        for (int i = 0; i < 4; i++) { bad[i] = (g[i].z != t0) | (g[i].w != t1); any |= bad[i]; }
        if (!__any(any)) break;
        FENCE();
#pragma unroll
        for (int i = 0; i < 4; i++) if (bad[i]) g[i] = ld128(rws, kvo, gs(i));
        DRAINV();
      }
    };
    SCHED();
    cs = v4f{0.f, 0.f, 0.f, 0.f};
    colsum(0, 10);
    cs0 = opq(cs.x);
    SCHED();
    DRAINV();
    if (w < 4) {
      poll4([](int i) { return (u32)(i * 64 * 14336); });
      const u32 la = L_RX + ((tid >> 1) & 1) * RXC + (508 - 8 * (tid >> 2) - 4 * (tid & 1)) * 2;
#pragma unroll
      for (int i = 0; i < 4; i++) *LDSP(v2u, la + (3 - i) * 1024) = v2u{g[i].x, g[i].y};
    } else {
      SCHED();
      poll4([&](int i) { return ((i + rq) & 3) * 4096; });
      const u32 xq = xo - 32 * 7168;
      sfor<0, 4>([&](auto I) { ald32s(x0[I.value], xq, rwv, ((I.value + rq) & 3) * 32 * 7168); });
    }
    const float dval = ch ? hi2f(dv2) : lo2f(dv2);
    const u32 sel = ch ? 0x03020c0cu : 0x01000c0cu;
    const v4u rw = mkv(w < 4 ? gw1 : gw3, D * F * 2);
    auto ldb = [&](int s, int q) { return *LDSP(v4u, ba + s * 64 + (7 - q) * 512); };
    auto xa = [&](u32 x, v4u st) { return v2u{x, pk(opq(fmaf(lo2f(x), u2f(st.y), u2f(st.x))), opq(fmaf(hi2f(x), u2f(st.y), u2f(st.x))))}; };
    auto r2 = [&](auto I) { constexpr u32 i = I.value; dma16<wslot(i), i * 57344u>(L_W + w * 1024, vo, rw); };
    BAR();
#pragma unroll
    for (int q = 0; q < 4; q++) { B[0][q] = ldb(0, q); B[1][q] = ldb(1, q); }
    const v2u kv0 = *LDSP(v2u, L_RX + ch * RXC + (2044 - tl) * 2), kv1 = *LDSP(v2u, L_RX + ch * RXC + (1020 - tl) * 2);
    SCHED();
    v4u sg0, sg1;
    const u32 r1q = (rq & 1) * 512;
    ald128so<true>(sg0, so, rwv, r1q * 16);
    ald128so<true>(sg1, so, rwv, (512 - r1q) * 16);
    sfor<0, NR2A>([&](auto I) { r2(IC(10 + I.value)); });
    v4f acc[8];
#pragma unroll
    for (int n = 0; n < 8; n++) acc[n] = v4f{0.f, 0.f, 0.f, 0.f};
    SCHED();
    sfor<0, 20>([&](auto I) {
      constexpr int i = I, q = i < 8 ? 0 : i < 14 ? 1 : i < 18 ? 2 : 3, r = i - (q == 0 ? 0 : q == 1 ? 8 : q == 2 ? 14 : 18), s = r / (4 - q), m = r % (4 - q);
      acc[m + q] = MFMA16(A[s][m], B[s][q], acc[m + q]);
      if constexpr (i == 13) { A[0][7] = lda(0, 7); B[0][7] = ldb(0, 7); }
      if constexpr (i == 17) { A[0][6] = lda(0, 6); B[0][6] = ldb(0, 6); }
      if constexpr (i % 3 == 2 && NR2A + i / 3 < 10) r2(IC(10 + NR2A + i / 3));
      if constexpr (i & 1) SCHED();
    });
    static_assert(NR2A + 6 >= 10, "all of the early second half is issued in the first part");
    constexpr int PM[26] = {1, 2, 3, 7, 2, 3, 0, 3, 6, 6, 0, 1, 5, 5, 5, 0, 1, 2, 4, 4, 4, 4, 0, 1, 2, 3};
    constexpr int PG[26] = {3, 3, 3, 0, 2, 2, 7, 1, 0, 1, 6, 6, 0, 1, 2, 5, 5, 5, 0, 1, 2, 3, 4, 4, 4, 4};
    auto redw = [&](u32 base, int n0) {
#pragma unroll
      for (int n = 0; n < 4; n++)
        if (n != wq) *LDSP(v4f, base + rdw_ + (n < wq ? n : n - 1) * 1024) = acc[n0 + n];
    };
    auto own = [&](int n0) { v4f y; sfor<0, 4>([&](auto N) { if (N.value == wq) y = acc[n0 + N.value]; }); return y; };
    auto hout = [&](v4f y, v4u xa_, v4u xb_, v2u kv, u32 off) {
      auto ex = [&](u32 v) { return u2f(__builtin_amdgcn_perm(v, v, sel)); };
      auto one = [&](u32 a, float k, float yy, u32 x) { return opq(fmaf(ex(a), opq(fmaf(k, dval, yy)), ex(x))); };
      const u32 h0 = canon(pk(one(xa_.y, hi2f(kv.y), y.x, xa_.x), one(xa_.w, lo2f(kv.y), y.y, xa_.z)));
      const u32 h1 = canon(pk(one(xb_.y, hi2f(kv.x), y.z, xb_.x), one(xb_.w, lo2f(kv.x), y.w, xb_.z)));
      const auto r0 = __builtin_amdgcn_permlane16_swap(h0, h0, false, false), r1 = __builtin_amdgcn_permlane16_swap(h1, h1, false, false);
      st128(v4u{h0, h1, r0[1], r1[1]}, rws, off);
    };
    auto sum4 = [&](v4f y, v4f* pr) {
      v4f r;
#pragma unroll
      for (int e = 0; e < 4; e++) r[e] = opq(opq(y[e] + pr[0][e]) + opq(pr[1][e] + pr[2][e]));
      return r;
    };
    asm volatile("s_waitcnt vmcnt(%6)" : "+v"(sg0), "+v"(sg1), "+v"(x0[0]), "+v"(x0[1]), "+v"(x0[2]), "+v"(x0[3]) : "n"(10) : "memory");
    fixg(sg0, rws, so + r1q * 16, t0, t1);
    fixg(sg1, rws, so + (512 - r1q) * 16, t0, t1);
    u32 x1a, x1b;
    ald32s(x1a, xo, rwv, (128 + r1q / 8) * 7168);
    ald32s(x1b, xo, rwv, (192 - r1q / 8) * 7168);
    if (w >= 4) {
#pragma unroll
      for (int i = 0; i < 4; i++) *LDSP(v2u, L_SX0 + (tid - 256) * 8 + ((i + rq) & 3) * 2048) = xa(x0[i], g[i]);
    }
    redw(L_RED0, 0);
    v4f y = own(0);
    BAR();
    {
      v4f pr[3];
#pragma unroll
      for (int q = 0; q < 3; q++) pr[q] = *LDSP(v4f, L_RED0 + rdr[q] + lane * 16);
      const v4u sa = *LDSP(v4u, L_SX0 + tl * 8), sb = *LDSP(v4u, L_SX0 + tl * 8 + 16);
      SCHED();
      sfor<0, 26>([&](auto I) {
        constexpr int i = I;
        if constexpr (i == 0) { A[0][5] = lda(0, 5); B[0][5] = ldb(0, 5); }
        if constexpr (i == 3) { A[0][4] = lda(0, 4); B[0][4] = ldb(0, 4); }
        if constexpr (i >= 12 && i < 20) { constexpr int j = 7 - ((i - 12) >> 1); if (i & 1) B[1][j] = ldb(1, j); else A[1][j] = lda(1, j); }
        acc[PM[i] + PG[i]] = MFMA16(A[0][PM[i]], B[0][PG[i]], acc[PM[i] + PG[i]]);
        if constexpr (i == 2) y = sum4(y, pr);
        if constexpr (i == 4) { FENCE(); hout(y, sa, sb, kv0, ho); FENCE(); }
        SCHED();
      });
    }
    asm volatile("s_waitcnt vmcnt(%2)" : "+v"(x1a), "+v"(x1b) : "n"(1) : "memory");
    BAR();
    u32 cn;
    sfor<0, 26>([&](auto I) {
      constexpr int i = I;
      if constexpr (i < 5) { w13[19 - 2 * i] = trd2<1024>(wra + (9 - 2 * i) * 8192); w13[18 - 2 * i] = trd2<1024>(wra + (8 - 2 * i) * 8192); }
      if constexpr (i == 1) lsig<L_CNT + 12>();
      if constexpr (i >= 5 && i < 10) r2(IC(20 + i - 5));
      if constexpr (i == R3A - 2) { cn = *LDSP(volatile u32, L_CNT + 12); FENCE(); }
      if constexpr (i == R3A) { if (UNLIKELY(__builtin_amdgcn_readfirstlane(cn) < 8u)) { lwait(L_CNT + 12); } FENCE(); }
      if constexpr (i >= R3A && i < R3A + 3) r2(IC(25 + i - R3A));
      if constexpr (i == XAT + 1) *LDSP(v2u, L_SX1 + (r1q + tid) * 8) = xa(x1a, sg0);
      if constexpr (i == XAT + 2) *LDSP(v2u, L_SX1 + (512 - r1q + tid) * 8) = xa(x1b, sg1);
      acc[PM[i] + PG[i]] = MFMA16(A[1][PM[i]], B[1][PG[i]], acc[PM[i] + PG[i]]);
      SCHED();
    });
    redw(L_RED1, 4);
    y = own(4);
    BAR();
    sfor<0, 7>([&](auto I) { dma16s<I.value * 8192u, I.value * 8192u>(w * 1024, hdo, rwv); });
    {
      v4f pr[3];
#pragma unroll
      for (int q = 0; q < 3; q++) pr[q] = *LDSP(v4f, L_RED1 + rdr[q] + lane * 16);
      const v4u sa = *LDSP(v4u, L_SX1 + tl * 8), sb = *LDSP(v4u, L_SX1 + tl * 8 + 16);
      y = sum4(y, pr);
      hout(y, sa, sb, kv1, ho + 16 * 57344);
    }
    FENCE();
    FENCE();
    SCHED();
    cs = v4f{0.f, 0.f, 0.f, 0.f};
    colsum(10, 20);
    asm volatile("" : "+v"(cs));
  }
  FENCE();
  PHASE();

  u32 hva = (u32)(w * 1024 + lane * 16 + ((lane >> 3) & 3) * 4);
  OPQ(hva);
  auto hfix = [&](auto hf_, u32 m) {
    constexpr u32 HF = decltype(hf_)::value;
    if (UNLIKELY(__any(m == 0xFFFFFFFFu))) {
#pragma nounroll
      for (u32 spin = 0; spin < SPIN_MAX; spin++) {
        sfor<0, 7>([&](auto I) { dma16s<HF * L_H + I.value * 8192u, HF * 16 * 57344u + I.value * 8192u>(w * 1024, hdo, rwv); });
        WAITV(0);
        u32 k = 0;
#pragma unroll
        for (int i = 0; i < 7; i++) k = max(k, *LDSP(u32, HF * L_H + hva + i * 8192));
        if (!__any(k == 0xFFFFFFFFu)) break;
      }
    }
  };
  {
    u32 m = 0;
    waitv<4>();
#pragma unroll
    for (int i = 0; i < 4; i++) m = max(m, *LDSP(u32, hva + (i) * 8192));
    waitv<1>();
#pragma unroll
    for (int i = 4; i < 7; i++) m = max(m, *LDSP(u32, hva + (i) * 8192));
    hfix(IC(0), m);
  }
  const int hh = lane >> 5;
  u32 trb = hh * 1024 + ((lane & 15) >> 2) * 64 + ((((lane >> 4) & 1) ^ hh) * 32) + (lane & 3) * 8;
  u32 w2o = (u32)((224 * w + (lane >> 3)) * 896 + dt * 128 + ((lane & 7) ^ ((lane >> 3) & 2)) * 16);
  u32 w2a = (u32)((lane >> 4) * 1024 + ((lane & 15) >> 2) * 128 + (((lane >> 3) & 1) | ((lane >> 3) & 2)) * 32 + (lane & 3) * 8);
  OPQ(trb); OPQ(w2o); OPQ(w2a);
  const u32 fbo = WS_FB + rt * 128 + (lane < 14 ? lane : 0) * 8;
  const v4u rw2 = mkv(gw2, F * D * 2);
  const u32 slA = L_SA + w * 4096, slB = w * 4096, slC = w < 6 ? 32768 + w * 4096 : L_SC + (w - 6) * 4096;
  const u32 h2o = WS_HF + (u32)(((rt * 2 + q2) * 4 * 56 + 7 * w) * 1024 + lane * 16);
  const u32 sto = WS_S2 + (u32)(64 * rt + lane) * 16;
  auto lin = [](int i) -> u32 { return (i) * 1024u; };
  v4u wa[7][4];
  auto rdw = [&](int t, u32 sl) {
    const u32 a = w2a + sl;
#pragma unroll
    for (int j = 0; j < 4; j++) wa[t][j] = trd2<512>(a ^ (32u * j));
  };
  auto usew = [&](int t) { asm volatile("" ::"v"(wa[t][0]), "v"(wa[t][1]), "v"(wa[t][2]), "v"(wa[t][3])); };
  auto rdres = [&]() {
    return trd2<256>((u32)((w >> 2) * L_H + (8 * dt + 2 * (lane >> 4) + ((lane >> 1) & 1)) * 1024 + ((w >> 1) & 1) * 512 + ((lane & 15) >> 2) * 64 +
                           ((q2 ^ ((lane >> 1) & 1)) * 32) + 16 * (w & 1) + 8 * (lane & 1)));
  };
  v4u hf[4][7], res, bfr[112], sg, wc;
  v2u fbl;
  v16f ac[4];
  u32 cnt, hm, nm[4], hq[7], fs;
  float cv[4], ny, hv[8], ee[8];
  const v16f zero16 = {};
  auto center = [&](int t, v16f c) { return MFMA32(wc, (v4u{nm[t], nm[t], nm[t], nm[t]}), c); };
  auto stats = [&](int t) {
    fixg(sg, rws, sto + t * 8192, t0, t1);
    const auto a = __builtin_amdgcn_permlane32_swap(sg.x, sg.x, false, false), c = __builtin_amdgcn_permlane32_swap(sg.y, sg.y, false, false);
    nm[t] = a[0]; nm[t + 1] = a[1]; cv[t] = u2f(c[0]); cv[t + 1] = u2f(c[1]);
  };
  auto lchk = [&](u32 a) { if (UNLIKELY(__builtin_amdgcn_readfirstlane(cnt) < 8u)) lwait(a); FENCE(); };
  constexpr int PD = PDX;
  auto ldf = [&](int T) { bfr[T] = trd2<256>(trb + ((T / 28) & 1) * 512 + (T % 28) * 2048); };
  BAR();
#pragma unroll
  for (int T = 0; T < PD; T++) ldf(T);
  SCHED();
  sfor<0, 112>([&](auto T_) {
    constexpr int T = T_, nt = T / 28, s = T % 28;
    if constexpr (T == HD0 - 2 || T == 54 - PD || T == T_W[4][0] - 2) { cnt = *LDSP(volatile u32, L_CNT + (T == HD0 - 2 ? 4 : T == 54 - PD ? 8 : 16)); FENCE(); }
    if constexpr (T == HD0) lchk(L_CNT + 4);
    if constexpr (T == T_W[4][0]) lchk(L_CNT + 16);
    if constexpr (T == 56 - PD) {
      lchk(L_CNT + 8);
      res = rdres();
      lsig<L_CNT + 16>();
      trb += L_H;
      OPQ(trb);
    }
    if constexpr (T + PD < 112) ldf(T + PD);
    if constexpr (find(T_RD, T) >= 0) {
      constexpr int t = find(T_RD, T);
      waitv<nvm(T_W[t][3], T)>();
      rdw(t, t < 4 ? slA : t == 4 ? slB : slC);
    }
    if constexpr (T < 8) w13[20 + T] = trd2<1024>(wra + wslot(20 + T));
    if constexpr (T == T_HV) { waitv<nvm(T_HD[6], T)>(); }
    if constexpr (T == T_HV) {
      u32 a = hva + L_H;
      OPQ(a);
#pragma unroll
      for (int i = 0; i < 7; i++) hq[i] = *LDSP(u32, a + i * 8192);
    }
    if constexpr (T == T_HV + 2) hm = max(max(max(hq[0], hq[1]), max(hq[2], hq[3])), max(max(hq[4], hq[5]), hq[6]));
    if constexpr (T == 8) lsig<L_CNT + 4>();
    if constexpr (T == T_HV + 2) { hfix(IC(1), hm); lsig<L_CNT + 8>(); }
    if constexpr (T == T_HF[0]) {
      waitv<nvm(T_FB, T)>(fbl);
      if (UNLIKELY(__any((fbl.x != t0) | (fbl.y != t1)))) {
#pragma nounroll
        for (u32 spin = 0; spin < SPIN_MAX; spin++) {
          FENCE();
          fbl = ld64(rws, fbo, 0);
          if (!__any((fbl.x != t0) | (fbl.y != t1))) break;
        }
        DRAINV();
      }
      FENCE();
    }
    if constexpr (T == T_EP[0] || T == 84) { waitv<nvm(T_G[T == 84], T)>(sg); stats(T == 84 ? 2 : 0); }
    if constexpr (T == T_FS) lsigr<L_CNT + 24>(fs);
    if constexpr (s == 0) ac[nt] = nt == 3 ? center(nt, zero16) : zero16;
    ac[nt] = MFMA32(w13[s], bfr[T], ac[nt]);
    if constexpr (T == T_EP[0]) { ac[0] = center(0, ac[0]); ac[1] = center(1, ac[1]); }
    if constexpr (T == 84) ac[2] = center(2, ac[2]);
    if constexpr (T == 10) colsum(20, 24);
    if constexpr (T == 13) colsum(24, 28);
    if constexpr (T == 16) {
      const float c = cs0 + cs.x;
      const u32 h = pk(c, c);
      wc = v4u{h, pk(c - lo2f(h), 0.f), 0u, 0u};
      OPQ(wc);
    }
    if constexpr (T == T_FB) ald64s(fbl, fbo, rwv);
    if constexpr (find(T_PZ, T) >= 0) { FENCE(); st128(poison, rws, hfo + find(T_PZ, T) * 57344u); FENCE(); }
    if constexpr (find(T_G, T) >= 0) ald128so<true>(sg, sto, rwv, find(T_G, T) * 16384);
    if constexpr (T == T_FL) { OPQ(fs); st64(t0, t1, rws, fs == 7u ? WS_FB + rt * 128 + fj * 8 : OOB); FENCE(); }
    if constexpr (findw(T) >= 0) {
      constexpr int t = findw(T) / 4, i = findw(T) % 4;
      if constexpr (i == 0 && (t == 1 || t == 2 || t == 3)) usew(t - 1);
      if constexpr (i == 0 && t == 6) usew(3);
      dma16<i * 1024u, t * 28672u + i * 7168u>(t == 4 ? slB : t == 5 ? slC : slA, (i & 1) ? w2o ^ 64u : w2o, rw2);
    }
    if constexpr (find(T_HD, T) >= 0) { constexpr u32 i = find(T_HD, T);
      dma16s<L_H + i * 8192u, 16 * 57344u + i * 8192u>(w * 1024, hdo, rwv); }
    if constexpr (find(T_HF, T) >= 0) { constexpr int i = find(T_HF, T); FENCE(); hf[i / 7][i % 7] = __builtin_amdgcn_raw_buffer_load_b128(rws, h2o, ((i / 7) * 56 + (i % 7)) * 1024, SC1); FENCE(); }
    if constexpr (T == PRI0 || T == PWA) { if (w >= 4) __builtin_amdgcn_s_setprio(1); }
    if constexpr (T == PWB) __builtin_amdgcn_s_setprio(0);
    if constexpr (nt == 3 && FAST2) {
      constexpr int e = T - T_EP[nt - 1];
      if constexpr (e == 1) ny = opq(-1.4426950408889634f * __builtin_amdgcn_rsqf(cv[nt - 1]));
      if constexpr (e >= 2 && e < 10) { ee[e - 2] = opq(ac[nt - 1][e - 2] * ny); hv[e - 2] = opq(ac[nt - 1][e - 2] * ac[nt - 1][e + 6]); }
      if constexpr (e >= 3 && e < 11) ee[e - 3] = opq(__builtin_amdgcn_exp2f(ee[e - 3]));
      if constexpr (e >= 4 && e < 12) ee[e - 4] = opq(__builtin_fmaf(ee[e - 4], cv[nt - 1], cv[nt - 1]));
      if constexpr (e >= 5 && e < 13) ee[e - 5] = opq(__builtin_amdgcn_rcpf(ee[e - 5]));
      if constexpr (e >= 6 && e < 14) hv[e - 6] = opq(hv[e - 6] * ee[e - 6]);
      if constexpr (e == 14) { FENCE(); st128(v4u{canon(pk(hv[0], hv[1])), pk(hv[2], hv[3]), pk(hv[4], hv[5]), pk(hv[6], hv[7])}, rws, hfo); FENCE(); }
      if constexpr (e == 15) { hfo += 56 * 1024; OPQ(hfo); }
    } else if constexpr (nt > 0) {
      constexpr int e = T - T_EP[nt - 1], r = (e - 2) >> 1;
      if constexpr (e == 1) ny = opq(-1.4426950408889634f * __builtin_amdgcn_rsqf(cv[nt - 1]));
      if constexpr (e >= 2 && e < 22 && (e & 1) == 0) {
        if constexpr (r < 8) { ee[r] = opq(ac[nt - 1][r] * ny); hv[r] = opq(ac[nt - 1][r] * ac[nt - 1][r + 8]); }
        if constexpr (r >= 1 && r < 9) ee[r - 1] = opq(__builtin_fmaf(ee[r - 1], cv[nt - 1], cv[nt - 1]));
        if constexpr (r >= 2) hv[r - 2] = opq(hv[r - 2] * ee[r - 2]);
      }
      if constexpr (e >= 2 && e < 20 && (e & 1) == 1) {
        if constexpr (r < 8) ee[r] = opq(__builtin_amdgcn_exp2f(ee[r]));
        if constexpr (r >= 1) ee[r - 1] = opq(__builtin_amdgcn_rcpf(ee[r - 1]));
      }
      if constexpr (e == 20) { FENCE(); st128(v4u{canon(pk(hv[0], hv[1])), pk(hv[2], hv[3]), pk(hv[4], hv[5]), pk(hv[6], hv[7])}, rws, hfo); FENCE(); }
      if constexpr (e == 21) { hfo += 56 * 1024; OPQ(hfo); }
    }
    SCHED();
  });
  static_assert(HD0 > 10 && T_HV > T_HD[6] && T_HV + 2 < 54 - PD && T_W[4][0] > 58 - PD && T_FL > T_FS + PD + 1 && T_PZ[3] < T_G[0] && EP0 < 7 && T_G[0] < 28 && T_G[1] > T_EP[0] && T_G[1] < 84, "order of the hand-offs");
  {
    ny = opq(-1.4426950408889634f * __builtin_amdgcn_rsqf(cv[3]));
    SCHED();
#pragma unroll
    for (int r = 0; r < 8; r++) ee[r] = opq(ac[3][r] * ny);
#pragma unroll
    for (int r = 0; r < 8; r++) { ee[r] = opq(__builtin_amdgcn_exp2f(ee[r])); hv[r] = opq(ac[3][r] * ac[3][r + 8]); }
#pragma unroll
    for (int r = 0; r < 8; r++) ee[r] = opq(__builtin_fmaf(ee[r], cv[3], cv[3]));
#pragma unroll
    for (int r = 0; r < 8; r++) ee[r] = opq(__builtin_amdgcn_rcpf(ee[r]));
#pragma unroll
    for (int r = 0; r < 8; r++) hv[r] = opq(hv[r] * ee[r]);
    waitv<nvm(T_W[6][3], 112)>();
    rdw(6, slA);
    FENCE();
    st128(v4u{canon(pk(hv[0], hv[1])), pk(hv[2], hv[3]), pk(hv[4], hv[5]), pk(hv[6], hv[7])}, rws, hfo);
  }
  FENCE();
  PHASE();
  u32 pw0 = (u32)(w * 4096 + lane * 16), pw1 = pw0 ^ 128u;
  OPQ(pw0); OPQ(pw1);
  __builtin_amdgcn_s_setprio(0);
  v4f c[2][4];
  const v4f zero4 = {};
  auto part = [&](int g_, int i) { *LDSP(v4f, ((i & 1) ? pw1 : pw0) + (g_ & 1) * 32768 + i * 1024) = c[g_ & 1][i]; };
  SCHED();
  sfor<0, 112>([&](auto U_) {
    constexpr int U = U_, g_ = U / 28, r = U % 28, s = r / 4, i = r % 4;
    c[g_ & 1][i] = MFMA16(wa[s][i], hf[g_][s], s == 0 ? zero4 : c[g_ & 1][i]);
    if constexpr (U >= HF2U && U < HF2U + 7 * HF2D && (U - HF2U) % HF2D == 0) { FENCE(); hf[2][(U - HF2U) / HF2D] = __builtin_amdgcn_raw_buffer_load_b128(rws, h2o, (2 * 56 + ((U - HF2U) / HF2D)) * 1024, SC1); FENCE(); }
    if constexpr (U >= HF3U && U < HF3U + 7 * HF3D && (U - HF3U) % HF3D == 0) { FENCE(); hf[3][(U - HF3U) / HF3D] = __builtin_amdgcn_raw_buffer_load_b128(rws, h2o, (3 * 56 + ((U - HF3U) / HF3D)) * 1024, SC1); FENCE(); }
    if constexpr (U == 56) { cnt = *LDSP(volatile u32, L_CNT + 20); FENCE(); }
    if constexpr (U == 59) lchk(L_CNT + 20);
    if constexpr (U == 87) { pw0 += 65536; pw1 += 65536; OPQ(pw0); OPQ(pw1); }
    if constexpr (g_ > 0 && r >= 3 && r < 11 && (r & 1)) part(g_ - 1, (r - 3) >> 1);
    if constexpr (U == 2) lsig<L_CNT + 20>();
    if constexpr (r == 27) {
      if (settle<7>(hf[g_], rws, h2o + g_ * 56 * 1024, lin)) {
#pragma unroll
        for (int k = 0; k < 28; k++) c[g_ & 1][k & 3] = MFMA16(wa[k >> 2][k & 3], hf[g_][k >> 2], k < 4 ? zero4 : c[g_ & 1][k & 3]);
      }
    }
    SCHED();
  });
  static_assert(HF3U >= 28 && HF2U + 6 * HF2D < 56 && HF3U + 6 * HF3D < 84, "");
  {
    const int n = 8 * (w & 1) + (lane & 7);
    u32 a = (u32)((w >> 1) * 32768 + (lane >> 3) * 512 + (n ^ ((lane >> 1) & 8)) * 16);
    u32 oo = (u32)((((w >> 2) * 1024 + 64 * rt + 32 * ((w >> 1) & 1) + 16 * q2 + n) * D + 64 * dt + 8 * (lane >> 3)) * 2);
    OPQ(a); OPQ(oo);
    const rsrc_t ro = mk(gout, S * D * 2);
#pragma unroll
    for (int i = 0; i < 4; i++) part(3, i);
    v4f e = {lo2f(res.x), hi2f(res.x), lo2f(res.y), hi2f(res.y)}, o = {lo2f(res.z), hi2f(res.z), lo2f(res.w), hi2f(res.w)};
    OPQ(e); OPQ(o);
    BAR();
    v4f pe[8], po[8];
#pragma unroll
    for (int u = 0; u < 8; u++) { pe[u] = *LDSP(v4f, a + u * 4096); po[u] = *LDSP(v4f, a + u * 4096 + 256); }
    SCHED();
    e = ((pe[0] + pe[1]) + (pe[2] + pe[3])) + ((pe[4] + pe[5]) + (pe[6] + (pe[7] + e)));
    o = ((po[0] + po[1]) + (po[2] + po[3])) + ((po[4] + po[5]) + (po[6] + (po[7] + o)));
    __builtin_amdgcn_raw_buffer_store_b128(v4u{pk(e.x, e.y), pk(e.z, e.w), pk(o.x, o.y), pk(o.z, o.w)}, ro, oo, 0, 17);
  }
  if (b == 0 && tid == 0) st64(t0, t1 ^ 0x5bd1e995u, rws, WS_EPOCH);
}

extern "C" void kernel_launch(void* const* d_in, const int* in_sizes, int n_in, void* d_out, int out_size, void* d_ws, size_t ws_size,
                              hipStream_t stream) {
  (void)hipFuncSetAttribute((const void*)fused_kernel, hipFuncAttributeMaxDynamicSharedMemorySize, L_TOTAL);
  fused_kernel<<<256, 512, L_TOTAL, stream>>>((const u16*)d_in[0], (const u16*)d_in[1], (const u16*)d_in[2], (const u16*)d_in[3],
                                              (const u16*)d_in[4], (const u16*)d_in[5], (const u16*)d_in[7], (const u16*)d_in[8],
                                              (const u16*)d_in[9], (u16*)d_out, (unsigned char*)d_ws);
}
```
